```python
import torch

def _softmax(x, axis=-1):
    return torch.softmax(x, dim=axis)
def _gelu(x):
    return torch.nn.functional.gelu(x, approximate="tanh")
def _silu(x):
    return torch.nn.functional.silu(x)
def _sigmoid(x):
    return torch.sigmoid(x)
def _layernorm(x, axis=-1, eps=1e-5):
    m = x.mean(dim=axis, keepdim=True)
    v = x.var(dim=axis, keepdim=True, unbiased=False)
    return (x - m) / torch.sqrt(v + eps)
def _scatter_add(tgt, idx, src, axis=0):
    return tgt.clone().index_add_(axis, idx, src)
def _segment_sum(src, seg, n):
    out = torch.zeros((n,) + src.shape[1:], dtype=src.dtype)
    return out.index_add_(0, seg, src)
def _pad(x, pw):
    flat = tuple(v for pair in reversed(pw) for v in pair)
    return torch.nn.functional.pad(x, flat)

kernel_name = 'gated_mix__gqa_block_cached__dense_activation_silu__decode'

def setup_inputs():

    _w = (lambda a: (lambda t: t.to(torch.bfloat16) if t.is_floating_point() else t)(torch.as_tensor(a)))
    def _A_setup():
        B, Sq, D = 2, 1, 448
        Hq, Hkv, Sk, Dh = 16, 8, 32768, 256
        assert Hq % Hkv == 0, (Hq, Hkv)
        return {
            "x": _w((lambda *s: torch.randn(*s, dtype=torch.bfloat16) * 0.05)(B, Sq, D)),
            "k_cache": _w((lambda *s: torch.randn(*s, dtype=torch.bfloat16) * 6.4)(B, Hkv, Sk, Dh)),
            "v_cache": _w((lambda *s: torch.randn(*s, dtype=torch.bfloat16) * 0.8)(B, Hkv, Sk, Dh)),
            "wq": _w((lambda *s: torch.randn(*s, dtype=torch.bfloat16) * 6.4)(D, Hq * Dh) * (D ** -0.5)),
            "wo": _w((lambda *s: torch.randn(*s, dtype=torch.bfloat16) * 0.8)(Hq * Dh, D) * ((Hq * Dh) ** -0.5)),
        }
    _A_in = _A_setup()

    def _B_setup():
        B, S, D, F = 2, 1, 448, 1792
        return {
            "x": _w((lambda *s: torch.randn(*s, dtype=torch.bfloat16) * 0.05)(B, S, D)),
            "w1": _w((lambda *s: torch.randn(*s, dtype=torch.bfloat16) * 12.8)(D, F) * (D ** -0.5)),
            "w2": _w((lambda *s: torch.randn(*s, dtype=torch.bfloat16) * 0.2)(F, D) * (F ** -0.5)),
        }
    _B_in = _B_setup()

    return {
        "A_x": _A_in["x"],
        "A_k_cache": _A_in["k_cache"],
        "A_v_cache": _A_in["v_cache"],
        "A_wq": _A_in["wq"],
        "A_wo": _A_in["wo"],
        "B_x": _B_in["x"],
        "B_w1": _B_in["w1"],
        "B_w2": _B_in["w2"],
        "gate_w": _w((lambda *s: torch.randn(*s, dtype=torch.bfloat16) * 0.2)(448) * 16.0),
    }

def reference(A_x, A_k_cache, A_v_cache, A_wq, A_wo, B_x, B_w1, B_w2, gate_w):
    def _A_ref(x, k_cache, v_cache, wq, wo):
        B, Sq, D = x.shape
        Hq, Dh = 16, 256
        Hkv = k_cache.shape[1]
        rep = Hq // Hkv
        q = (lambda x, *d: x.permute(*d))((x @ wq).reshape(B, Sq, Hq, Dh), 0, 2, 1, 3)
        k = (lambda x, n, axis: torch.repeat_interleave(x, n, dim=axis))(k_cache, rep, axis=1)
        v = (lambda x, n, axis: torch.repeat_interleave(x, n, dim=axis))(v_cache, rep, axis=1)
        scores = torch.einsum("bhqd,bhkd->bhqk", q, k) * (Dh ** -0.5)
        p = _softmax(scores, axis=-1)
        o = torch.einsum("bhqk,bhkd->bhqd", p, v)
        return (lambda x, *d: x.permute(*d))(o, 0, 2, 1, 3).reshape(B, Sq, Hq * Dh) @ wo

    def _B_ref(x, w1, w2):
        h = torch.einsum("bsd,df->bsf", x, w1)
        h = _silu(h)
        out = torch.einsum("bsf,fd->bsd", h, w2)
        return out

    x = A_x
    g = _sigmoid((x * gate_w).sum(-1, keepdims=True))
    a = _A_ref(x, A_k_cache, A_v_cache, A_wq, A_wo)
    b = _B_ref(x, B_w1, B_w2)
    return g * a + (1 - g) * b
```

```cpp
#include <hip/hip_runtime.h>
#include <hip/hip_bf16.h>
#include <stdint.h>

typedef unsigned u32x4 __attribute__((ext_vector_type(4)));
typedef float f32x4 __attribute__((ext_vector_type(4)));
typedef __attribute__((ext_vector_type(2))) __bf16 bf16x2;
#define GAS __attribute__((address_space(1)))

constexpr int D = 448, HQ = 16, HKV = 8, SK = 32768, DH = 256, F = 1792, NB = 2;
constexpr int NSPLIT = 16;
constexpr size_t WS_Q = 0;
constexpr size_t WS_H = 16384;
constexpr size_t WS_G = WS_H + 2 * 1792 * 4;
constexpr size_t WS_ACC = 65536;
constexpr size_t WS_PML = 131072;
constexpr size_t WS_PO = 262144;

__device__ __forceinline__ float bf2f(unsigned short b) { return __uint_as_float((unsigned)b << 16); }
__device__ __forceinline__ unsigned short f2bf(float f) { unsigned u = __float_as_uint(f); u += 0x7FFFu + ((u >> 16) & 1u); return (unsigned short)(u >> 16); }
__device__ __forceinline__ float rbf(float f) { return bf2f(f2bf(f)); }
__device__ __forceinline__ float dot2(unsigned k, unsigned q, float acc) {
  return __builtin_amdgcn_fdot2_f32_bf16(__builtin_bit_cast(bf16x2, k), __builtin_bit_cast(bf16x2, q), acc, false);
}
template <int CTRL> __device__ __forceinline__ float dpp(float x) {
  return __builtin_bit_cast(float, __builtin_amdgcn_mov_dpp(__builtin_bit_cast(int, x), CTRL, 0xf, 0xf, true));
}
constexpr int XOR1 = 0xB1, XOR2 = 0x4E, XOR7 = 0x141, XOR8 = 0x128, ROR4 = 0x124;

__global__ void __launch_bounds__(256) k_prep(const unsigned short* __restrict__ xa, const unsigned short* __restrict__ wq,
                                              const unsigned short* __restrict__ xb, const unsigned short* __restrict__ w1,
                                              const unsigned short* __restrict__ gw, unsigned char* __restrict__ ws) {
  __shared__ float red[32][2][64 + 1];
  __shared__ float xs[2][D];
  const int blk = blockIdx.x, t = threadIdx.x;
  if (blk == 92) {
    float* acc = (float*)(ws + WS_ACC);
    for (int i = t; i < 4 * D; i += 256) acc[i] = 0.f;
    if (t < 128) {
      const int b = t >> 6, l = t & 63;
      float s = 0.f;
      for (int k = l; k < D; k += 64) s += rbf(bf2f(xa[b * D + k]) * bf2f(gw[k]));
      for (int m = 32; m >= 1; m >>= 1) s += __shfl_xor(s, m);
      s = rbf(s);
      if (l == 0) ((float*)(ws + WS_G))[b] = rbf(1.f / (1.f + __expf(-s)));
    }
    return;
  }
  const bool isq = blk < 64;
  const unsigned short* x = isq ? xa : xb;
  const unsigned short* W = isq ? wq : w1;
  const int N = isq ? HQ * DH : F;
  const int c0 = (isq ? blk : blk - 64) * 64;
  for (int i = t; i < 2 * D; i += 256) xs[i / D][i % D] = bf2f(x[i]);
  __syncthreads();
  const int cg = t & 7, rg = t >> 3;
  float a0[8], a1[8];
  for (int j = 0; j < 8; ++j) a0[j] = a1[j] = 0.f;
  for (int r = rg; r < D; r += 32) {
    const u32x4 w = *(const u32x4*)(W + (size_t)r * N + c0 + cg * 8);
    const float x0 = xs[0][r], x1 = xs[1][r];
    for (int j = 0; j < 4; ++j) {
      const float lo = __uint_as_float(w[j] << 16), hi = __uint_as_float(w[j] & 0xffff0000u);
      a0[2 * j] += x0 * lo; a0[2 * j + 1] += x0 * hi; a1[2 * j] += x1 * lo; a1[2 * j + 1] += x1 * hi;
    }
  }
  for (int j = 0; j < 8; ++j) { red[rg][0][cg * 8 + j] = a0[j]; red[rg][1][cg * 8 + j] = a1[j]; }
  __syncthreads();
  if (t < 128) {
    const int b = t >> 6, c = t & 63;
    float s = 0.f;
    for (int r = 0; r < 32; ++r) s += red[r][b][c];
    s = rbf(s);
    if (isq) ((unsigned short*)(ws + WS_Q))[b * 4096 + c0 + c] = f2bf(s);
    else { const float sg = s / (1.f + __expf(-s)); ((float*)(ws + WS_H))[b * F + c0 + c] = rbf(sg); }
  }
}

__global__ void __launch_bounds__(512) k_attn(const unsigned short* __restrict__ kc, const unsigned short* __restrict__ vc, unsigned char* __restrict__ ws) {
  __shared__ __attribute__((aligned(16))) float lds[8 * 64 + 8 * 2 * (256 + 4)];
  const int g = blockIdx.x >> 4, sp = blockIdx.x & 15;
  const int b = g >> 3, kvh = g & 7;
  const int tid = threadIdx.x, lane = tid & 63, wave = __builtin_amdgcn_readfirstlane(tid >> 6);
  const int c = lane & 31, par = lane >> 5;
  const unsigned short* q = (const unsigned short*)(ws + WS_Q) + b * 4096 + kvh * 512;
  const u32x4 q0 = *(const u32x4*)(q + 8 * c), q1 = *(const u32x4*)(q + 256 + 8 * c);
  const size_t base = ((size_t)g * SK + (size_t)sp * 2048) * DH;
  const u32x4* kb = (const u32x4*)(kc + base) + lane;
  const u32x4* vb = (const u32x4*)(vc + base) + lane;
  constexpr float C2 = 1.4426950408889634f;
  float m = -INFINITY, l = 0.f;
  float acc[2][8];
  for (int h = 0; h < 2; ++h) for (int k = 0; k < 8; ++k) acc[h][k] = 0.f;
  float* pl = lds + wave * 64;
  u32x4 Kn[8], Vn[8];
  {
    const size_t o = (size_t)wave * 512;
#pragma unroll
    for (int i = 0; i < 8; ++i) Kn[i] = __builtin_nontemporal_load(kb + o + i * 64);
#pragma unroll
    for (int i = 0; i < 8; ++i) Vn[i] = __builtin_nontemporal_load(vb + o + i * 64);
  }
  for (int t = wave; t < 128; t += 8) {
    float x[16];
#pragma unroll
    for (int i = 0; i < 8; ++i) {
      float d0 = 0.f, d1 = 0.f;
#pragma unroll
      for (int k = 0; k < 4; ++k) { d0 = dot2(Kn[i][k], q0[k], d0); d1 = dot2(Kn[i][k], q1[k], d1); }
      x[2 * i] = d0; x[2 * i + 1] = d1;
    }
    const bool more = t + 8 < 128;
    const size_t on = (size_t)(t + 8) * 512;
    if (more) {
#pragma unroll
    for (int i = 0; i < 8; ++i) Kn[i] = __builtin_nontemporal_load(kb + on + i * 64);
    }
#pragma unroll
    for (int j = 0; j < 8; ++j) {
      auto s = __builtin_amdgcn_permlane16_swap(__float_as_uint(x[j]), __float_as_uint(x[j + 8]), false, false);
      x[j] = __uint_as_float(s[0]) + __uint_as_float(s[1]);
    }
#define TR_STEP(HALF, CTRL, BIT) _Pragma("unroll") for (int h = 0; h < (HALF); ++h) { \
      const float lo_ = x[h], hi_ = x[h + (HALF)]; const float t1_ = lo_ + dpp<CTRL>(lo_), t2_ = hi_ + dpp<CTRL>(hi_); \
      x[h] = (lane & (BIT)) ? t2_ : t1_; }
    TR_STEP(4, XOR8, 8) TR_STEP(2, XOR7, 4) TR_STEP(1, XOR2, 2)
#undef TR_STEP
    float sc = x[0] + dpp<XOR1>(x[0]);
    sc = rbf(sc) * 0.0625f * C2;
    float pm = sc;
    pm = fmaxf(pm, dpp<ROR4>(pm)); pm = fmaxf(pm, dpp<XOR8>(pm));
    { auto s = __builtin_amdgcn_permlane16_swap(__float_as_uint(pm), __float_as_uint(pm), false, false); pm = fmaxf(__uint_as_float(s[0]), __uint_as_float(s[1]));
      auto u = __builtin_amdgcn_permlane32_swap(__float_as_uint(pm), __float_as_uint(pm), false, false); pm = fmaxf(__uint_as_float(u[0]), __uint_as_float(u[1])); }
    const float mn = fmaxf(m, pm);
    const float alpha = __builtin_amdgcn_exp2f(m - mn), p = __builtin_amdgcn_exp2f(sc - mn);
    l = fmaf(l, alpha, (lane & 1) ? 0.f : p); m = mn;
    const float al0 = dpp<0x00>(alpha), al1 = dpp<0xAA>(alpha);
#pragma unroll
    for (int k = 0; k < 8; ++k) { acc[0][k] *= al0; acc[1][k] *= al1; }
    if (!(lane & 1)) pl[par * 16 + ((lane >> 1) & 15)] = p;
    asm volatile("s_waitcnt lgkmcnt(0)" ::: "memory");
    f32x4 pr[4];
#pragma unroll
    for (int u = 0; u < 4; ++u) pr[u] = *(const f32x4*)(pl + par * 16 + 4 * u);
#pragma unroll
    for (int i = 0; i < 8; ++i) {
      const float p0 = pr[i >> 1][(i & 1) * 2], p1 = pr[i >> 1][(i & 1) * 2 + 1];
#pragma unroll
      for (int k = 0; k < 4; ++k) {
        const float lo = __uint_as_float(Vn[i][k] << 16), hi = __uint_as_float(Vn[i][k] & 0xffff0000u);
        acc[0][2 * k] = fmaf(p0, lo, acc[0][2 * k]); acc[0][2 * k + 1] = fmaf(p0, hi, acc[0][2 * k + 1]);
        acc[1][2 * k] = fmaf(p1, lo, acc[1][2 * k]); acc[1][2 * k + 1] = fmaf(p1, hi, acc[1][2 * k + 1]);
      }
    }
    if (more) {
#pragma unroll
    for (int i = 0; i < 8; ++i) Vn[i] = __builtin_nontemporal_load(vb + on + i * 64);
    }
  }
#pragma unroll
  for (int h = 0; h < 2; ++h)
#pragma unroll
    for (int k = 0; k < 8; ++k) { auto u = __builtin_amdgcn_permlane32_swap(__float_as_uint(acc[h][k]), __float_as_uint(acc[h][k]), false, false); acc[h][k] = __uint_as_float(u[0]) + __uint_as_float(u[1]); }
  { float t = l; t += dpp<XOR1>(t); t += dpp<ROR4>(t); t += dpp<XOR8>(t);
    auto s = __builtin_amdgcn_permlane16_swap(__float_as_uint(t), __float_as_uint(t), false, false); t = __uint_as_float(s[0]) + __uint_as_float(s[1]);
    auto u = __builtin_amdgcn_permlane32_swap(__float_as_uint(t), __float_as_uint(t), false, false); l = __uint_as_float(u[0]) + __uint_as_float(u[1]); }
  float* mrg = lds + 8 * 64;
  if (par == 0) {
#pragma unroll
    for (int h = 0; h < 2; ++h) {
      float* rec = mrg + (wave * 2 + h) * 260;
      *(f32x4*)(rec + 4 + 8 * c) = f32x4{acc[h][0], acc[h][1], acc[h][2], acc[h][3]};
      *(f32x4*)(rec + 4 + 8 * c + 4) = f32x4{acc[h][4], acc[h][5], acc[h][6], acc[h][7]};
      if (c == 2 * h) { rec[0] = m; rec[1] = l; }
    }
  }
  __syncthreads();
  {
    const int h = tid >> 8, d = tid & 255;
    float M = -INFINITY;
    for (int w = 0; w < 8; ++w) M = fmaxf(M, mrg[(w * 2 + h) * 260]);
    float L = 0.f, o = 0.f;
    for (int w = 0; w < 8; ++w) { const float* rec = mrg + (w * 2 + h) * 260; const float wt = __builtin_amdgcn_exp2f(rec[0] - M); L = fmaf(wt, rec[1], L); o = fmaf(wt, rec[4 + d], o); }
    ((float*)(ws + WS_PO))[((size_t)blockIdx.x * 2 + h) * 256 + d] = o;
    if (d == 0) { float* pml = (float*)(ws + WS_PML) + ((size_t)blockIdx.x * 2 + h) * 2; pml[0] = M; pml[1] = L; }
  }
}

__global__ void __launch_bounds__(448) k_comb(const unsigned short* __restrict__ wo, const unsigned short* __restrict__ w2, unsigned char* __restrict__ ws) {
  __shared__ float o[2][64];
  const int t = threadIdx.x;
  float* accum = (float*)(ws + WS_ACC);
  if (blockIdx.x >= 256) {
    const int f0 = (blockIdx.x - 256) * 64;
    if (t < 128) o[t >> 6][t & 63] = ((const float*)(ws + WS_H))[(t >> 6) * F + f0 + (t & 63)];
    __syncthreads();
    float s0 = 0.f, s1 = 0.f;
    for (int r = 0; r < 64; ++r) { const float w = bf2f(w2[(size_t)(f0 + r) * D + t]); s0 += o[0][r] * w; s1 += o[1][r] * w; }
    atomicAdd(accum + 2 * D + t, s0); atomicAdd(accum + 3 * D + t, s1);
    return;
  }
  const int g = blockIdx.x >> 4, j = blockIdx.x & 15, b = g >> 3, kvh = g & 7;
  const int h = j >> 3, d0 = (j & 7) * 32;
  if (t < 32) {
    const float* pml = (const float*)(ws + WS_PML);
    const float* po = (const float*)(ws + WS_PO);
    float M = -INFINITY;
    for (int s = 0; s < 16; ++s) M = fmaxf(M, pml[((g * 16 + s) * 2 + h) * 2]);
    float L = 0.f, a = 0.f;
    for (int s = 0; s < 16; ++s) { const float w = __builtin_amdgcn_exp2f(pml[((g * 16 + s) * 2 + h) * 2] - M); L = fmaf(w, pml[((g * 16 + s) * 2 + h) * 2 + 1], L); a = fmaf(w, po[((size_t)(g * 16 + s) * 2 + h) * 256 + d0 + t], a); }
    o[0][t] = rbf(a / L);
  }
  __syncthreads();
  float s = 0.f;
  const int r0 = kvh * 512 + h * 256 + d0;
  for (int r = 0; r < 32; ++r) s += o[0][r] * bf2f(wo[(size_t)(r0 + r) * D + t]);
  atomicAdd(accum + b * D + t, s);
}

__global__ void __launch_bounds__(448) k_fin(unsigned char* __restrict__ ws, unsigned short* __restrict__ out) {
  const int b = blockIdx.x, t = threadIdx.x;
  const float* accum = (const float*)(ws + WS_ACC);
  const float g = ((const float*)(ws + WS_G))[b];
  const float a = rbf(accum[b * D + t]), bb = rbf(accum[(2 + b) * D + t]);
  const float r = rbf(g * a) + rbf(rbf(1.f - g) * bb);
  out[b * D + t] = f2bf(r);
}

extern "C" void kernel_launch(void* const* d_in, const int* in_sizes, int n_in, void* d_out, int out_size, void* d_ws, size_t ws_size, hipStream_t stream) {
  const unsigned short* xa = (const unsigned short*)d_in[0];
  const unsigned short* kc = (const unsigned short*)d_in[1];
  const unsigned short* vc = (const unsigned short*)d_in[2];
  const unsigned short* wq = (const unsigned short*)d_in[3];
  const unsigned short* wo = (const unsigned short*)d_in[4];
  const unsigned short* xb = (const unsigned short*)d_in[5];
  const unsigned short* w1 = (const unsigned short*)d_in[6];
  const unsigned short* w2 = (const unsigned short*)d_in[7];
  const unsigned short* gw = (const unsigned short*)d_in[8];
  unsigned char* ws = (unsigned char*)d_ws;
  k_prep<<<93, 256, 0, stream>>>(xa, wq, xa, w1, gw, ws); (void)xb;
  k_attn<<<256, 512, 0, stream>>>(kc, vc, ws);
  k_comb<<<256 + 28, 448, 0, stream>>>(wo, w2, ws);
  k_fin<<<2, 448, 0, stream>>>(ws, (unsigned short*)d_out);
}
```

```cpp
#include <hip/hip_runtime.h>
#include <stdint.h>

#define LAS __attribute__((address_space(3)))
#define GAS __attribute__((address_space(1)))
typedef unsigned u32x4 __attribute__((ext_vector_type(4)));
typedef unsigned u32x2 __attribute__((ext_vector_type(2)));
typedef float f32x4 __attribute__((ext_vector_type(4)));
typedef short s16x4 __attribute__((ext_vector_type(4)));
typedef __bf16 bf16x8 __attribute__((ext_vector_type(8)));
typedef GAS unsigned long long gu64;
typedef GAS unsigned gu32;

#define WIN 24
#define STAMPS 0

constexpr int D = 448, NQ = 4096, FF = 1792, SK = 32768;
constexpr int NWG = 256, NTHR = 384, NTILE = 64;
constexpr unsigned L_RING = 0;
constexpr unsigned L_R = 131072;
constexpr unsigned L_X = L_R + 14336;
constexpr unsigned L_Q = 159744;
constexpr unsigned L_YB = 160768;
constexpr unsigned L_MISC = 162560;
constexpr unsigned L_LANDED = L_MISC, L_CONS = L_MISC + 16, L_QRDY = L_MISC + 32, L_TAG = L_MISC + 36;
constexpr unsigned L_ML = L_MISC + 64;
constexpr unsigned L_O16 = L_MISC + 128;
constexpr unsigned L_BYTES = L_MISC + 256;
static_assert(L_BYTES <= 163840, "LDS");
constexpr size_t W_EPOCH = 0;
constexpr size_t W_GQ = 4096;
constexpr size_t W_GA = 65536;
constexpr size_t W_GB = 2u << 20;
constexpr size_t W_DBG = 4u << 20;
constexpr unsigned long long TMO = 25000000ull;

__device__ __forceinline__ unsigned long long rt() { return __builtin_amdgcn_s_memrealtime(); }
__device__ __forceinline__ float bf2f(unsigned b) { return __uint_as_float(b << 16); }
__device__ __forceinline__ unsigned f2bf(float f) { unsigned u = __float_as_uint(f); u += 0x7FFFu + ((u >> 16) & 1u); return u >> 16; }
__device__ __forceinline__ float rbf(float f) { return bf2f(f2bf(f)); }
__device__ __forceinline__ unsigned cvtpk(float lo, float hi) { unsigned r; asm("v_cvt_pk_bf16_f32 %0, %1, %2" : "=v"(r) : "v"(lo), "v"(hi)); return r; }
template <int CTRL> __device__ __forceinline__ float dppf(float x) { return __builtin_bit_cast(float, __builtin_amdgcn_mov_dpp(__builtin_bit_cast(int, x), CTRL, 0xf, 0xf, true)); }
template <int CTRL> __device__ __forceinline__ unsigned dppu(unsigned x) { return (unsigned)__builtin_amdgcn_mov_dpp((int)x, CTRL, 0xf, 0xf, true); }
constexpr int XOR1 = 0xB1, XOR2 = 0x4E, ROR4 = 0x124, ROR8 = 0x128, HMIR = 0x141;
__device__ __forceinline__ float x16max(float x) { auto s = __builtin_amdgcn_permlane16_swap(__float_as_uint(x), __float_as_uint(x), false, false); return fmaxf(__uint_as_float(s[0]), __uint_as_float(s[1])); }
__device__ __forceinline__ float x32max(float x) { auto s = __builtin_amdgcn_permlane32_swap(__float_as_uint(x), __float_as_uint(x), false, false); return fmaxf(__uint_as_float(s[0]), __uint_as_float(s[1])); }
__device__ __forceinline__ float x16sum(float x) { auto s = __builtin_amdgcn_permlane16_swap(__float_as_uint(x), __float_as_uint(x), false, false); return __uint_as_float(s[0]) + __uint_as_float(s[1]); }
__device__ __forceinline__ float x32sum(float x) { auto s = __builtin_amdgcn_permlane32_swap(__float_as_uint(x), __float_as_uint(x), false, false); return __uint_as_float(s[0]) + __uint_as_float(s[1]); }
__device__ __forceinline__ f32x4 mfma(u32x4 a, u32x4 b, f32x4 c) { return __builtin_amdgcn_mfma_f32_16x16x32_bf16(__builtin_bit_cast(bf16x8, a), __builtin_bit_cast(bf16x8, b), c, 0, 0, 0); }
__device__ __forceinline__ u32x2 trrd(unsigned addr) { return __builtin_bit_cast(u32x2, __builtin_amdgcn_ds_read_tr16_b64_v4i16((LAS s16x4*)addr)); }
__device__ __forceinline__ u32x4 ld128(unsigned addr) { return *(const LAS u32x4*)addr; }
__device__ __forceinline__ u32x2 ld64(unsigned addr) { return *(const LAS u32x2*)addr; }
__device__ __forceinline__ unsigned ldv(unsigned addr) { return *(volatile LAS unsigned*)addr; }
__device__ __forceinline__ void stv(unsigned addr, unsigned v) { *(volatile LAS unsigned*)addr = v; }
__device__ __forceinline__ u32x4 mk_rsrc(const void* p, unsigned bytes) {
  const unsigned long long a = (unsigned long long)p;
  u32x4 r; r.x = (unsigned)a; r.y = (unsigned)(a >> 32) & 0xffffu; r.z = bytes; r.w = 0x00020000u; return r;
}
__device__ __forceinline__ void st_gran(gu64* g, unsigned tag, unsigned v) { __hip_atomic_store(g, ((unsigned long long)tag << 32) | v, __ATOMIC_RELAXED, __HIP_MEMORY_SCOPE_AGENT); }
__device__ __forceinline__ unsigned long long ld_gran(gu64* g) { return __hip_atomic_load(g, __ATOMIC_RELAXED, __HIP_MEMORY_SCOPE_AGENT); }
#define LGKM0() asm volatile("s_waitcnt lgkmcnt(0)" ::: "memory")
#define VM0() asm volatile("s_waitcnt vmcnt(0)" ::: "memory")
#define BAR() asm volatile("s_barrier" ::: "memory")

template <int N>
__device__ __forceinline__ void dma4(unsigned m0v, unsigned v0, unsigned v1, unsigned v2, unsigned v3, u32x4 rs, unsigned soff) {
  asm volatile("s_mov_b32 m0, %0\n\ts_nop 0\n\t"
               "buffer_load_dwordx4 %1, %5, %6 offen nt lds\n\t"
               "buffer_load_dwordx4 %2, %5, %6 offen offset:1024 nt lds\n\t"
               "buffer_load_dwordx4 %3, %5, %6 offen offset:2048 nt lds\n\t"
               "buffer_load_dwordx4 %4, %5, %6 offen offset:3072 nt lds\n\t"
               "s_waitcnt vmcnt(%7)"
               :: "s"(m0v), "v"(v0), "v"(v1), "v"(v2), "v"(v3), "s"(rs), "s"(soff), "i"(N) : "memory");
}
__device__ __forceinline__ void dma1(unsigned m0v, unsigned v0, u32x4 rs, unsigned soff) {
  asm volatile("s_mov_b32 m0, %0\n\ts_nop 0\n\tbuffer_load_dwordx4 %1, %2, %3 offen nt lds" :: "s"(m0v), "v"(v0), "s"(rs), "s"(soff) : "memory");
}

#define STAMP(i) do { } while (0)

__global__ void __launch_bounds__(NTHR, 2)
gated_mix_kernel(const unsigned short* __restrict__ xa, const unsigned short* __restrict__ kc, const unsigned short* __restrict__ vc,
                 const unsigned short* __restrict__ wq, const unsigned short* __restrict__ wo, const unsigned short* __restrict__ w1,
                 const unsigned short* __restrict__ w2, const unsigned short* __restrict__ gw, unsigned short* __restrict__ out,
                 unsigned char* __restrict__ ws) {
  extern __shared__ __attribute__((aligned(16))) unsigned char lds_raw[];
  const int tid = threadIdx.x, lane = tid & 63;
  const int wave = __builtin_amdgcn_readfirstlane(tid >> 6);
  const int wg = blockIdx.x;
  const int grp = wg >> 4, j = wg & 15, b = grp >> 3, kvh = grp & 7;
  (void)lds_raw;

  if (wave == 0) {
    STAMP(0);
    const u32x4 rk = mk_rsrc(kc, 256u << 20), rv = mk_rsrc(vc, 256u << 20);
    unsigned vk[8], vv[4];
    const unsigned hi = lane >> 5, pc = lane & 31;
#pragma unroll
    for (int i = 0; i < 8; ++i) vk[i] = hi * 512 + ((pc ^ (2 * i + hi)) << 4);
#pragma unroll
    for (int i = 0; i < 4; ++i) vv[i] = hi * 512 + ((pc ^ ((2 * i + hi) << 1)) << 4);
    const unsigned base = (unsigned)(grp * SK + j * 2048) * 512u;
    unsigned pub = 0;
    for (int t = 0; t < NTILE; ++t) {
      const unsigned slot = L_RING + (t & 3) * 32768u;
      if (t >= 4) {
        const unsigned need = t >> 2;
        if (__builtin_amdgcn_readfirstlane(ldv(L_CONS + (t & 3) * 4)) < need) {
          VM0();
          pub = 32u * t; if (lane == 0) stv(L_LANDED, pub);
          while (__builtin_amdgcn_readfirstlane(ldv(L_CONS + (t & 3) * 4)) < need) __builtin_amdgcn_s_sleep(1);
        }
      }
      const unsigned so = base + t * 16384u;
#define PUB(gi) if constexpr (((4 * (gi) + 8 - WIN) & 15) == 0) { const int cand = 32 * t + 4 * (gi) + 8 - WIN; if (cand > (int)pub) { pub = cand; if (lane == 0) stv(L_LANDED, pub); } }
      dma4<WIN - 4>(slot, vk[0], vk[1], vk[2], vk[3], rk, so); PUB(0)
      dma4<WIN - 4>(slot + 4096, vk[4], vk[5], vk[6], vk[7], rk, so + 4096); PUB(1)
      dma4<WIN - 4>(slot + 8192, vk[0], vk[1], vk[2], vk[3], rk, so + 8192); PUB(2)
      dma4<WIN - 4>(slot + 12288, vk[4], vk[5], vk[6], vk[7], rk, so + 12288); PUB(3)
      dma4<WIN - 4>(slot + 16384, vv[0], vv[1], vv[2], vv[3], rv, so); PUB(4)
      if (t == 0) BAR();
      dma4<WIN - 4>(slot + 20480, vv[0], vv[1], vv[2], vv[3], rv, so + 4096); PUB(5)
      dma4<WIN - 4>(slot + 24576, vv[0], vv[1], vv[2], vv[3], rv, so + 8192); PUB(6)
      dma4<WIN - 4>(slot + 28672, vv[0], vv[1], vv[2], vv[3], rv, so + 12288); PUB(7)
#undef PUB
    }
    asm volatile("s_waitcnt vmcnt(16)" ::: "memory");
    if (lane == 0) stv(L_LANDED, 32u * NTILE - 16u);
    VM0();
    if (lane == 0) stv(L_LANDED, 32u * NTILE);
    STAMP(1);
    LGKM0();
    BAR();
    return;
  }

  if (wave >= 2) {
    const int c = wave - 2;
    const unsigned g = lane >> 4, r = lane & 15;
    BAR();
    const unsigned slot = L_RING + c * 32768u;
    unsigned ka[4], va[8];
#pragma unroll
    for (int s = 0; s < 4; ++s) ka[s] = slot + r * 512 + (((4 * s + g) ^ r) << 4);
    {
      const unsigned q4 = r >> 2, p = lane & 3, row = 4 * g + q4;
      const unsigned X = (row & 7) << 1;
#pragma unroll
      for (int n = 0; n < 8; ++n) va[n] = slot + 16384 + row * 512 + (((2 * n + (p >> 1)) ^ X) << 4) + (p & 1) * 8;
    }
    while (__builtin_amdgcn_readfirstlane(ldv(L_QRDY)) == 0) __builtin_amdgcn_s_sleep(1);
    u32x4 qf[8];
#pragma unroll
    for (int s = 0; s < 8; ++s) qf[s] = ld128(L_Q + (lane & 1) * 512 + s * 64 + g * 16);
    f32x4 acc[16];
#pragma unroll
    for (int n = 0; n < 16; ++n) acc[n] = f32x4{0.f, 0.f, 0.f, 0.f};
    float m = -INFINITY, l = 0.f;
    constexpr float C2 = 0.0625f * 1.4426950408889634f;
    for (int it = 0; it < NTILE / 4; ++it) {
      const unsigned t = c + 4 * it;
      while (__builtin_amdgcn_readfirstlane(ldv(L_LANDED)) < 32 * t + 16) __builtin_amdgcn_s_sleep(1);
      f32x4 s0 = {0.f, 0.f, 0.f, 0.f}, s1 = {0.f, 0.f, 0.f, 0.f};
      {
        u32x4 kf[16];
#pragma unroll
        for (int s = 0; s < 8; ++s) { kf[s] = ld128(ka[s & 3] + (s >> 2) * 256); kf[8 + s] = ld128(ka[s & 3] + (s >> 2) * 256 + 8192); }
#pragma unroll
        for (int s = 0; s < 8; ++s) { s0 = mfma(kf[s], qf[s], s0); s1 = mfma(kf[8 + s], qf[s], s1); }
      }
      float tm = fmaxf(fmaxf(fmaxf(s0[0], s0[1]), fmaxf(s0[2], s0[3])), fmaxf(fmaxf(s1[0], s1[1]), fmaxf(s1[2], s1[3])));
      tm = x32max(x16max(tm)) * C2;
      if (__any(tm > m)) {
        const float mn = fmaxf(m, tm);
        const float alpha = __builtin_amdgcn_exp2f(m - mn);
        m = mn; l *= alpha;
#pragma unroll
        for (int n = 0; n < 16; ++n) acc[n] *= alpha;
      }
      float p[8];
#pragma unroll
      for (int i = 0; i < 4; ++i) { p[i] = __builtin_amdgcn_exp2f(fmaf(s0[i], C2, -m)); p[4 + i] = __builtin_amdgcn_exp2f(fmaf(s1[i], C2, -m)); }
      l += ((p[0] + p[1]) + (p[2] + p[3])) + ((p[4] + p[5]) + (p[6] + p[7]));
      u32x4 pf; pf.x = cvtpk(p[0], p[1]); pf.y = cvtpk(p[2], p[3]); pf.z = cvtpk(p[4], p[5]); pf.w = cvtpk(p[6], p[7]);
      while (__builtin_amdgcn_readfirstlane(ldv(L_LANDED)) < 32 * t + 32) __builtin_amdgcn_s_sleep(1);
      u32x2 vf[32];
#pragma unroll
      for (int n = 0; n < 16; ++n) { vf[2 * n] = trrd(va[n & 7] + (n >> 3) * 256); vf[2 * n + 1] = trrd(va[n & 7] + (n >> 3) * 256 + 8192); }
      LGKM0();
      if (lane == 0) stv(L_CONS + c * 4, it + 1);
#pragma unroll
      for (int n = 0; n < 16; ++n) { u32x4 a; a.x = vf[2 * n].x; a.y = vf[2 * n].y; a.z = vf[2 * n + 1].x; a.w = vf[2 * n + 1].y; acc[n] = mfma(a, pf, acc[n]); }
    }
    l = x32sum(x16sum(l));
    if (r < 2) {
#pragma unroll
      for (int n = 0; n < 16; ++n) *(LAS f32x4*)(slot + r * 1024 + (16 * n + 4 * g) * 4) = acc[n];
      if (g == 0) { *(LAS float*)(L_ML + (c * 2 + r) * 8) = m; *(LAS float*)(L_ML + (c * 2 + r) * 8 + 4) = l; }
    }
    LGKM0();
    BAR();
    {
      const unsigned tag = ldv(L_TAG);
      const int d = tid - 128;
      gu64* ga = (gu64*)(ws + W_GA) + (size_t)grp * 16 * 16 * 48 + (size_t)j * 48;
      float Mh[2], Lh[2];
#pragma unroll
      for (int h = 0; h < 2; ++h) {
        float mw[4], M = -INFINITY;
#pragma unroll
        for (int w = 0; w < 4; ++w) { mw[w] = *(const LAS float*)(L_ML + (w * 2 + h) * 8); M = fmaxf(M, mw[w]); }
        float L = 0.f, o = 0.f;
#pragma unroll
        for (int w = 0; w < 4; ++w) {
          const float wt = __builtin_amdgcn_exp2f(mw[w] - M);
          L = fmaf(wt, *(const LAS float*)(L_ML + (w * 2 + h) * 8 + 4), L);
          o = fmaf(wt, *(const LAS float*)(L_RING + w * 32768 + h * 1024 + d * 4), o);
        }
        Mh[h] = M; Lh[h] = L;
        st_gran(ga + (size_t)(h * 8 + (d >> 5)) * (16 * 48) + (d & 31), tag, __float_as_uint(o));
      }
      if (d < 32) {
        const int cj = d >> 1, h = cj >> 3;
        const float v = (d & 1) ? (h ? Lh[1] : Lh[0]) : (h ? Mh[1] : Mh[0]);
        st_gran(ga + (size_t)cj * (16 * 48) + 32 + (d & 1), tag, __float_as_uint(v));
      }
    }
    return;
  }

  {
    STAMP(2);
    const unsigned g = lane >> 4, r = lane & 15, q4 = r >> 2, p = lane & 3;
    const unsigned epoch = __hip_atomic_load((gu32*)(ws + W_EPOCH), __ATOMIC_RELAXED, __HIP_MEMORY_SCOPE_AGENT);
    const u32x4 x0 = *((const GAS u32x4*)xa + lane);
    const u32x4 x1 = *((const GAS u32x4*)xa + 64 + (lane < 48 ? lane : 47));
    const u32x4 gwv = *((const GAS u32x4*)gw + (lane < 56 ? lane : 55));
    const int cs = (wg & 7) * 32 + (wg >> 3);
    {
      const u32x4 rq = mk_rsrc(wq, D * NQ * 2);
      const unsigned vo = (lane >> 1) * (NQ * 2) + (lane & 1) * 16;
#pragma unroll
      for (int i = 0; i < 14; ++i) dma1(L_R + i * 1024, vo, rq, cs * 32 + i * 32 * (NQ * 2));
    }
    if (lane < 16) stv(L_MISC + lane * 4, 0u);
    LGKM0();
    BAR();
    asm volatile("s_waitcnt vmcnt(14)" ::: "memory");
    *(LAS u32x4*)(L_X + lane * 16) = x0;
    if (lane < 48) *(LAS u32x4*)(L_X + 1024 + lane * 16) = x1;
    const unsigned tag = __builtin_amdgcn_readfirstlane(epoch) + 1u;
    if (lane == 0) stv(L_TAG, tag);
    VM0();
    LGKM0();
    STAMP(3);
    const unsigned xa_addr = L_X + (lane & 1) * 896 + g * 8;
    const unsigned wt_addr = L_R + (4 * g + q4) * 32 + p * 8;
    auto slice_gemv = [&]() -> f32x4 {
      f32x4 a = {0.f, 0.f, 0.f, 0.f};
#pragma unroll
      for (int s = 0; s < 14; ++s) {
        const u32x2 a0 = ld64(xa_addr + s * 64), a1 = ld64(xa_addr + s * 64 + 32);
        const u32x2 b0 = trrd(wt_addr + s * 1024), b1 = trrd(wt_addr + s * 1024 + 512);
        a = mfma(u32x4{a0.x, a0.y, a1.x, a1.y}, u32x4{b0.x, b0.y, b1.x, b1.y}, a);
      }
      return a;
    };
    {
      const f32x4 a = slice_gemv();
      const unsigned bits = f2bf((g & 1) ? a[1] : a[0]);
      const unsigned nb = dppu<XOR1>(bits);
      if (g < 2 && !(lane & 1)) st_gran((gu64*)(ws + W_GQ) + g * 2048 + cs * 8 + (r >> 1), tag, bits | (nb << 16));
    }
    STAMP(4);
    {
      gu64* gq = (gu64*)(ws + W_GQ) + b * 2048 + kvh * 256 + lane;
      const unsigned long long t0 = rt();
      unsigned v[4];
      for (;;) {
        bool ok = true;
#pragma unroll
        for (int i = 0; i < 4; ++i) { const unsigned long long x = ld_gran(gq + i * 64); v[i] = (unsigned)x; ok &= (unsigned)(x >> 32) == tag; }
        if (__all(ok)) break;
        if (rt() - t0 > TMO) break;
      }
#pragma unroll
      for (int i = 0; i < 4; ++i) *(LAS unsigned*)(L_Q + (i * 64 + lane) * 4) = v[i];
      LGKM0();
      if (lane == 0) stv(L_QRDY, 1u);
    }
    STAMP(5);
    const int fidx = kvh * 2 + (j >> 3);
    float yb[8];
#pragma unroll
    for (int k = 0; k < 8; ++k) yb[k] = 0.f;
    if (fidx < 14) {
      const int fs = (j & 7) * 14 + fidx;
      const u32x4 r1 = mk_rsrc(w1, D * FF * 2);
      const unsigned vo = (lane >> 1) * (FF * 2) + (lane & 1) * 16;
#pragma unroll
      for (int i = 0; i < 14; ++i) dma1(L_R + i * 1024, vo, r1, fs * 32 + i * 32 * (FF * 2));
      u32x4 w2r[16];
      const GAS u32x4* w2p = (const GAS u32x4*)(w2 + (size_t)fs * 16 * D) + (lane < 56 ? lane : 55);
#pragma unroll
      for (int i = 0; i < 16; ++i) w2r[i] = __builtin_nontemporal_load(w2p + i * 56);
      VM0();
      const f32x4 a = slice_gemv();
      float hv = rbf(b ? a[1] : a[0]);
      hv = rbf(hv / (1.f + __expf(-hv)));
#pragma unroll
      for (int i = 0; i < 16; ++i) {
        const float hs = __builtin_bit_cast(float, __builtin_amdgcn_readlane(__builtin_bit_cast(int, hv), i));
#pragma unroll
        for (int k = 0; k < 4; ++k) { yb[2 * k] = fmaf(hs, bf2f(w2r[i][k] & 0xffffu), yb[2 * k]); yb[2 * k + 1] = fmaf(hs, __uint_as_float(w2r[i][k] & 0xffff0000u), yb[2 * k + 1]); }
      }
    }
    float gate, omg;
    {
      const u32x4 xv = ld128(L_X + b * 896 + (lane < 56 ? lane : 55) * 16);
      float s = 0.f;
#pragma unroll
      for (int k = 0; k < 4; ++k) { s += rbf(bf2f(xv[k] & 0xffffu) * bf2f(gwv[k] & 0xffffu)); s += rbf(__uint_as_float(xv[k] & 0xffff0000u) * __uint_as_float(gwv[k] & 0xffff0000u)); }
      if (lane >= 56) s = 0.f;
      s += dppf<XOR1>(s); s += dppf<XOR2>(s); s += dppf<HMIR>(s); s += dppf<ROR8>(s); s = x32sum(x16sum(s));
      s = rbf(s);
      gate = rbf(1.f / (1.f + __expf(-s)));
      omg = rbf(1.f - gate);
    }
    if (lane < 56) {
      *(LAS f32x4*)(L_YB + lane * 32) = f32x4{yb[0] * omg, yb[1] * omg, yb[2] * omg, yb[3] * omg};
      *(LAS f32x4*)(L_YB + lane * 32 + 16) = f32x4{yb[4] * omg, yb[5] * omg, yb[6] * omg, yb[7] * omg};
    }
    LGKM0();
    {
      const u32x4 ro = mk_rsrc(wo, NQ * D * 2);
      const unsigned vo = (lane >> 1) * (D * 2) + (lane & 1) * 16;
      const unsigned r0 = (kvh * 512 + j * 32) * (D * 2);
#pragma unroll
      for (int i = 0; i < 28; ++i) dma1(L_R + i * 1024, vo, ro, r0 + i * 32);
    }
    VM0();
    STAMP(6);
    BAR();
    STAMP(7);
    float oval;
    {
      const unsigned hf = lane >> 5, d = lane & 31;
      gu64* ga = (gu64*)(ws + W_GA) + ((size_t)grp * 16 + j) * (16 * 48);
      gu64* gv = ga + hf * 8 * 48 + d;
      gu64* gm = ga + (hf * 8 + (lane & 7)) * 48 + 32;
      const unsigned long long t0 = rt();
      unsigned v[8], mm, ll;
      for (;;) {
        bool ok = true;
#pragma unroll
        for (int i = 0; i < 8; ++i) { const unsigned long long x = ld_gran(gv + i * 48); v[i] = (unsigned)x; ok &= (unsigned)(x >> 32) == tag; }
        { const unsigned long long x = ld_gran(gm); mm = (unsigned)x; ok &= (unsigned)(x >> 32) == tag; }
        { const unsigned long long x = ld_gran(gm + 1); ll = (unsigned)x; ok &= (unsigned)(x >> 32) == tag; }
        if (__all(ok)) break;
        if (rt() - t0 > TMO) break;
      }
      STAMP(8);
      const float Mp = __uint_as_float(mm), Lp = __uint_as_float(ll);
      float M = Mp; M = fmaxf(M, dppf<XOR1>(M)); M = fmaxf(M, dppf<XOR2>(M)); M = fmaxf(M, dppf<HMIR>(M)); M = x32max(M);
      const float wt = __builtin_amdgcn_exp2f(Mp - M);
      float L = wt * Lp; L += dppf<XOR1>(L); L += dppf<XOR2>(L); L += dppf<HMIR>(L); L = x32sum(L);
      const float sc = wt / L;
      float o = 0.f;
      o = fmaf(dppf<0x150>(sc), __uint_as_float(v[0]), o); o = fmaf(dppf<0x151>(sc), __uint_as_float(v[1]), o);
      o = fmaf(dppf<0x152>(sc), __uint_as_float(v[2]), o); o = fmaf(dppf<0x153>(sc), __uint_as_float(v[3]), o);
      o = fmaf(dppf<0x154>(sc), __uint_as_float(v[4]), o); o = fmaf(dppf<0x155>(sc), __uint_as_float(v[5]), o);
      o = fmaf(dppf<0x156>(sc), __uint_as_float(v[6]), o); o = fmaf(dppf<0x157>(sc), __uint_as_float(v[7]), o);
      oval = x32sum(o);
    }
    if (lane < 32) *(LAS unsigned short*)(L_O16 + lane * 2) = (unsigned short)f2bf(oval);
    LGKM0();
    {
      const u32x2 a0 = ld64(L_O16 + g * 8), a1 = ld64(L_O16 + 32 + g * 8);
      const u32x4 af = {a0.x, a0.y, a1.x, a1.y};
      gu64* gb = (gu64*)(ws + W_GB) + (size_t)b * 112 * 128 * 4 + (size_t)(wg & 127) * 4;
#pragma unroll
      for (int k = 0; k < 7; ++k) {
        float y[4];
#pragma unroll
        for (int u = 0; u < 4; ++u) {
          const int ct = 4 * k + u;
          const u32x2 b0 = trrd(wt_addr + ct * 1024), b1 = trrd(wt_addr + ct * 1024 + 512);
          const f32x4 dd = mfma(af, u32x4{b0.x, b0.y, b1.x, b1.y}, f32x4{0.f, 0.f, 0.f, 0.f});
          y[u] = dd[0];
        }
        const float ys = g == 0 ? y[0] : g == 1 ? y[1] : g == 2 ? y[2] : y[3];
        const unsigned n = 64 * k + lane;
        const float yv = fmaf(gate, ys, *(const LAS float*)(L_YB + n * 4));
        st_gran(gb + (size_t)(n >> 2) * 512 + (n & 3), tag, __float_as_uint(yv));
      }
    }
    STAMP(9);
    const int fp = wg & 127;
    if (fp < 112) {
      gu64* gb = (gu64*)(ws + W_GB) + ((size_t)b * 112 + fp) * 512 + lane;
      const unsigned long long t0 = rt();
      unsigned v[8];
      for (;;) {
        bool ok = true;
#pragma unroll
        for (int i = 0; i < 8; ++i) { const unsigned long long x = ld_gran(gb + i * 64); v[i] = (unsigned)x; ok &= (unsigned)(x >> 32) == tag; }
        if (__all(ok)) break;
        if (rt() - t0 > TMO) break;
      }
      float s = ((__uint_as_float(v[0]) + __uint_as_float(v[1])) + (__uint_as_float(v[2]) + __uint_as_float(v[3]))) +
                ((__uint_as_float(v[4]) + __uint_as_float(v[5])) + (__uint_as_float(v[6]) + __uint_as_float(v[7])));
      s += dppf<ROR4>(s); s += dppf<ROR8>(s); s = x32sum(x16sum(s));
      if (lane < 4) out[b * D + fp * 4 + lane] = (unsigned short)f2bf(s);
      if (wg == 0 && lane == 0) __hip_atomic_store((gu32*)(ws + W_EPOCH), tag, __ATOMIC_RELAXED, __HIP_MEMORY_SCOPE_AGENT);
    }
    STAMP(10);
  }
}

extern "C" void kernel_launch(void* const* d_in, const int* in_sizes, int n_in, void* d_out, int out_size, void* d_ws, size_t ws_size, hipStream_t stream) {
  (void)in_sizes; (void)n_in; (void)out_size; (void)ws_size;
  static bool attr = false;
  if (!attr) { (void)hipFuncSetAttribute((const void*)gated_mix_kernel, hipFuncAttributeMaxDynamicSharedMemorySize, L_BYTES); attr = true; }
  gated_mix_kernel<<<NWG, NTHR, L_BYTES, stream>>>((const unsigned short*)d_in[0], (const unsigned short*)d_in[1], (const unsigned short*)d_in[2],
                                                   (const unsigned short*)d_in[3], (const unsigned short*)d_in[4], (const unsigned short*)d_in[6],
                                                   (const unsigned short*)d_in[7], (const unsigned short*)d_in[8], (unsigned short*)d_out, (unsigned char*)d_ws);
}
```

```cpp
#include <hip/hip_runtime.h>
#include <stdint.h>
#include <type_traits>

#define LAS __attribute__((address_space(3)))
#define GAS __attribute__((address_space(1)))
typedef unsigned u32x4 __attribute__((ext_vector_type(4)));
typedef unsigned u32x2 __attribute__((ext_vector_type(2)));
typedef float f32x4 __attribute__((ext_vector_type(4)));
typedef short s16x4 __attribute__((ext_vector_type(4)));
typedef __bf16 bf16x8 __attribute__((ext_vector_type(8)));
typedef GAS unsigned long long gu64;
typedef GAS unsigned gu32;

#define WLO 16
#define WMID 20
#define WHI 28
#define THR 100
#define CAL_A 14
#define CAL_B 30
#define CAL_EPS 0.995f
#define PACE 1
#define T_Q 450
#define T_W1 700
#define T_WO 1000
#define STAGGER 6
#define PSLEEP 0
#define STAMPS 0

constexpr int D = 448, NQ = 4096, FF = 1792, SK = 32768;
constexpr int NWG = 256, NTHR = 384, NTILE = 64;
constexpr unsigned L_RING = 0;
constexpr unsigned L_R = 131072;
constexpr unsigned L_Q = 159744;
constexpr unsigned L_YB = 160768;
constexpr unsigned L_X = L_YB;
constexpr unsigned L_MISC = 162560;
constexpr unsigned L_LANDED = L_MISC, L_CONS = L_MISC + 16, L_QRDY = L_MISC + 32, L_HRDY = L_MISC + 36, L_TA = L_MISC + 40, L_TB = L_MISC + 44,
                   L_FIN = L_MISC + 48, L_DEL = L_MISC + 52, L_TKA = L_MISC + 56, L_DONE = L_MISC + 60;
constexpr unsigned L_TAG = L_MISC + 64, L_GATE = L_MISC + 68;
constexpr unsigned L_ML = L_MISC + 128;
constexpr unsigned L_O16 = L_MISC + 192;
constexpr unsigned L_BYTES = L_MISC + 256;
static_assert(L_BYTES <= 163840, "LDS");
constexpr size_t W_EPOCH = 0;
constexpr size_t W_GQ = 4096;
constexpr size_t W_GP = 40960;
constexpr unsigned W_ML = 65536;
constexpr unsigned W_GA = 131072;
constexpr unsigned W_GB = W_GA + (1u << 20);
constexpr unsigned W_END = W_GB + 2 * 112 * 128 * 32;
constexpr size_t W_DBG = 4u << 20;
constexpr unsigned long long TMO = 25000000ull;

#pragma clang diagnostic ignored "-Wint-to-pointer-cast"
__device__ __forceinline__ unsigned long long rt() { return __builtin_amdgcn_s_memrealtime(); }
__device__ __forceinline__ float bf2f(unsigned b) { return __uint_as_float(b << 16); }
__device__ __forceinline__ unsigned f2bf(float f) { unsigned u = __float_as_uint(f); u += 0x7FFFu + ((u >> 16) & 1u); return u >> 16; }
__device__ __forceinline__ float rbf(float f) { return bf2f(f2bf(f)); }
__device__ __forceinline__ unsigned cvtpk(float lo, float hi) { unsigned r; asm("v_cvt_pk_bf16_f32 %0, %1, %2" : "=v"(r) : "v"(lo), "v"(hi)); return r; }
template <int CTRL> __device__ __forceinline__ float dppf(float x) { return __builtin_bit_cast(float, __builtin_amdgcn_mov_dpp(__builtin_bit_cast(int, x), CTRL, 0xf, 0xf, true)); }
template <int CTRL> __device__ __forceinline__ unsigned dppu(unsigned x) { return (unsigned)__builtin_amdgcn_mov_dpp((int)x, CTRL, 0xf, 0xf, true); }
constexpr int XOR1 = 0xB1, XOR2 = 0x4E, ROR4 = 0x124, ROR8 = 0x128, HMIR = 0x141;
__device__ __forceinline__ float x16max(float x) { auto s = __builtin_amdgcn_permlane16_swap(__float_as_uint(x), __float_as_uint(x), false, false); return fmaxf(__uint_as_float(s[0]), __uint_as_float(s[1])); }
__device__ __forceinline__ float x32max(float x) { auto s = __builtin_amdgcn_permlane32_swap(__float_as_uint(x), __float_as_uint(x), false, false); return fmaxf(__uint_as_float(s[0]), __uint_as_float(s[1])); }
__device__ __forceinline__ float x16sum(float x) { auto s = __builtin_amdgcn_permlane16_swap(__float_as_uint(x), __float_as_uint(x), false, false); return __uint_as_float(s[0]) + __uint_as_float(s[1]); }
__device__ __forceinline__ float x32sum(float x) { auto s = __builtin_amdgcn_permlane32_swap(__float_as_uint(x), __float_as_uint(x), false, false); return __uint_as_float(s[0]) + __uint_as_float(s[1]); }
__device__ __forceinline__ f32x4 mfma(u32x4 a, u32x4 b, f32x4 c) { return __builtin_amdgcn_mfma_f32_16x16x32_bf16(__builtin_bit_cast(bf16x8, a), __builtin_bit_cast(bf16x8, b), c, 0, 0, 0); }
__device__ __forceinline__ u32x2 trrd(unsigned addr) { return __builtin_bit_cast(u32x2, __builtin_amdgcn_ds_read_tr16_b64_v4i16((LAS s16x4*)addr)); }
__device__ __forceinline__ u32x4 ld128(unsigned addr) { return *(const LAS u32x4*)addr; }
__device__ __forceinline__ u32x2 ld64(unsigned addr) { return *(const LAS u32x2*)addr; }
__device__ __forceinline__ float ldf(unsigned addr) { return *(const LAS float*)addr; }
__device__ __forceinline__ unsigned ldv(unsigned addr) { return *(volatile LAS unsigned*)addr; }
__device__ __forceinline__ unsigned ldu(unsigned addr) { return __builtin_amdgcn_readfirstlane(*(volatile LAS unsigned*)addr); }
__device__ __forceinline__ void stv(unsigned addr, unsigned v) { *(volatile LAS unsigned*)addr = v; }
__device__ __forceinline__ void st1(unsigned addr, unsigned v) {
  unsigned a, t;
  asm volatile("s_mov_b64 exec, 1\n\tv_mov_b32 %0, %2\n\tv_mov_b32 %1, %3\n\tds_write_b32 %0, %1\n\ts_mov_b64 exec, -1" : "=&v"(a), "=&v"(t) : "s"(addr), "s"(v) : "memory");
}
__device__ __forceinline__ unsigned opaque(unsigned x) { asm volatile("" : "+v"(x)); return x; }
__device__ __forceinline__ u32x4 mk_rsrc(const void* p, unsigned bytes) {
  const unsigned long long a = (unsigned long long)p;
  u32x4 r; r.x = (unsigned)a; r.y = (unsigned)(a >> 32) & 0xffffu; r.z = bytes; r.w = 0x00020000u; return r;
}
__device__ __forceinline__ void st_gran(gu64* g, unsigned tag, unsigned v) { __hip_atomic_store(g, ((unsigned long long)tag << 32) | v, __ATOMIC_RELAXED, __HIP_MEMORY_SCOPE_AGENT); }
__device__ __forceinline__ unsigned long long ld_gran(gu64* g) { return __hip_atomic_load(g, __ATOMIC_RELAXED, __HIP_MEMORY_SCOPE_AGENT); }
__device__ __forceinline__ __amdgpu_buffer_rsrc_t ws_rsrc(unsigned char* ws) { return __builtin_amdgcn_make_buffer_rsrc(ws, 0, W_END, 0x00020000); }
__device__ __forceinline__ void st_gran2(__amdgpu_buffer_rsrc_t rs, unsigned off, unsigned tag, float v0, float v1) {
  __builtin_amdgcn_raw_buffer_store_b128(u32x4{__float_as_uint(v0), tag, __float_as_uint(v1), tag}, rs, off, 0, 16);
}
__device__ __forceinline__ u32x4 ld_gran2(__amdgpu_buffer_rsrc_t rs, unsigned off) { return __builtin_amdgcn_raw_buffer_load_b128(rs, off, 0, 16); }
#define LGKM0() asm volatile("s_waitcnt lgkmcnt(0)" ::: "memory")
#define VM0() asm volatile("s_waitcnt vmcnt(0)" ::: "memory")
#define BAR() asm volatile("s_barrier" ::: "memory")
#define SCHED() __builtin_amdgcn_sched_barrier(0)
template <int N> __device__ __forceinline__ void vmwait() { asm volatile("s_waitcnt vmcnt(%0)" :: "i"(N) : "memory"); }

__device__ __forceinline__ void dma4(unsigned m0v, unsigned v0, unsigned v1, unsigned v2, unsigned v3, u32x4 rs, unsigned soff) {
  asm volatile("s_mov_b32 m0, %0\n\ts_nop 0\n\t"
               "buffer_load_dwordx4 %1, %5, %6 offen nt lds\n\t"
               "buffer_load_dwordx4 %2, %5, %6 offen offset:1024 nt lds\n\t"
               "buffer_load_dwordx4 %3, %5, %6 offen offset:2048 nt lds\n\t"
               "buffer_load_dwordx4 %4, %5, %6 offen offset:3072 nt lds"
               :: "s"(m0v), "v"(v0), "v"(v1), "v"(v2), "v"(v3), "s"(rs), "s"(soff) : "memory");
}
__device__ __forceinline__ void dma1(unsigned m0v, unsigned v0, u32x4 rs, unsigned soff) {
  asm volatile("s_mov_b32 m0, %0\n\ts_nop 0\n\tbuffer_load_dwordx4 %1, %2, %3 offen nt lds" :: "s"(m0v), "v"(v0), "s"(rs), "s"(soff) : "memory");
}

#define STAMP(i) do { } while (0)

__device__ __forceinline__ u32x4 qk_softmax(const u32x4 (&kf)[16], const u32x4 (&qf)[8], float& m, float& l, f32x4 (&acc)[16]) {
  constexpr float C2 = 0.0625f * 1.4426950408889634f;
  f32x4 s0 = {0.f, 0.f, 0.f, 0.f}, s1 = {0.f, 0.f, 0.f, 0.f};
#pragma unroll
  for (int s = 0; s < 8; ++s) { s0 = mfma(kf[s], qf[s], s0); s1 = mfma(kf[8 + s], qf[s], s1); }
  float tm = fmaxf(fmaxf(fmaxf(s0[0], s0[1]), fmaxf(s0[2], s0[3])), fmaxf(fmaxf(s1[0], s1[1]), fmaxf(s1[2], s1[3])));
  tm = x32max(x16max(tm)) * C2;
  if (__any(tm > m)) {
    const float mn = fmaxf(m, tm);
    const float alpha = __builtin_amdgcn_exp2f(m - mn);
    m = mn; l *= alpha;
#pragma unroll
    for (int n = 0; n < 16; ++n) acc[n] *= alpha;
  }
  float p[8];
#pragma unroll
  for (int i = 0; i < 4; ++i) { p[i] = __builtin_amdgcn_exp2f(fmaf(s0[i], C2, -m)); p[4 + i] = __builtin_amdgcn_exp2f(fmaf(s1[i], C2, -m)); }
  l += ((p[0] + p[1]) + (p[2] + p[3])) + ((p[4] + p[5]) + (p[6] + p[7]));
  u32x4 pf; pf.x = cvtpk(p[0], p[1]); pf.y = cvtpk(p[2], p[3]); pf.z = cvtpk(p[4], p[5]); pf.w = cvtpk(p[6], p[7]);
  return pf;
}

__global__ void __launch_bounds__(NTHR, 2)
gated_mix_kernel(const unsigned short* __restrict__ xa, const unsigned short* __restrict__ kc, const unsigned short* __restrict__ vc,
                 const unsigned short* __restrict__ wq, const unsigned short* __restrict__ wo, const unsigned short* __restrict__ w1,
                 const unsigned short* __restrict__ w2, const unsigned short* __restrict__ gw, unsigned short* __restrict__ out,
                 unsigned char* __restrict__ ws) {
  const int tid = threadIdx.x, lane = tid & 63;
  const int wave = __builtin_amdgcn_readfirstlane(tid >> 6);
  const int wg = blockIdx.x;
  const int grp = wg >> 4, j = wg & 15, b = grp >> 3, kvh = grp & 7;
  const unsigned cs = (wg & 7) * 32 + (wg >> 3);

  if (wave == 0) {
    STAMP(0);
    __builtin_amdgcn_s_setprio(3);
    {
      const u32x4 rq = mk_rsrc(wq, D * NQ * 2);
      const unsigned vo = (lane >> 1) * (NQ * 2) + (lane & 1) * 16;
#pragma unroll
      for (int i = 0; i < 14; ++i) dma1(L_R + i * 1024, vo, rq, cs * 32 + i * 32 * (NQ * 2));
    }
    const u32x4 rk = mk_rsrc(kc, 256u << 20), rv = mk_rsrc(vc, 256u << 20);
    unsigned vk[8], vv[4];
    const unsigned hi = lane >> 5, pc = lane & 31;
#pragma unroll
    for (int i = 0; i < 8; ++i) vk[i] = hi * 512 + ((pc ^ (2 * i + hi)) << 4);
#pragma unroll
    for (int i = 0; i < 4; ++i) vv[i] = hi * 512 + ((pc ^ ((2 * i + hi) << 1)) << 4);
    const unsigned base = (unsigned)(grp * SK + j * 2048) * 512u;
    int pub = 0;
    unsigned del = 0, fin = 0;
    int mode = 1, mode_next = 1;
    bool free_next = true;
    auto pick = [&](int u) {
      if (!PACE) return;
      if (u <= 2 * CAL_B + 1) {
        if (u == 2 * CAL_A + 1) st1(L_TA, (unsigned)rt() | 1u);
        if (u == 2 * CAL_B + 1) st1(L_TB, (unsigned)rt() | 1u);
        return;
      }
      if (!del) { del = ldu(L_DEL); fin = ldu(L_FIN); if (!del) return; }
      const int lag = (int)((unsigned)rt() - fin) + (int)(((2 * NTILE + 1 - u) * del) >> 4);
      mode_next = lag < -THR ? 0 : lag > THR ? 2 : 1;
    };
    auto half = [&](auto wc, auto vc_, int t, unsigned slot, unsigned so) {
      constexpr int W = decltype(wc)::value; constexpr bool ISV = decltype(vc_)::value;
#pragma unroll
      for (int gi = 0; gi < 4; ++gi) {
        vmwait<W - 4>();
        if constexpr (ISV) dma4(slot + 16384 + gi * 4096, vv[0], vv[1], vv[2], vv[3], rv, so + gi * 4096);
        else if (gi & 1) dma4(slot + gi * 4096, vk[4], vk[5], vk[6], vk[7], rk, so + gi * 4096);
        else dma4(slot + gi * 4096, vk[0], vk[1], vk[2], vk[3], rk, so + gi * 4096);
        if (((4 * gi - (W - 4)) & 15) == 0) {
          const int cand = 16 + 32 * t + (ISV ? 16 : 0) + 4 * gi - (W - 4);
          if (cand > pub) { pub = cand; st1(L_LANDED, (unsigned)cand); }
        }
        if (gi == 0) {
          if constexpr (!ISV) { if (t == 0) BAR(); }
          pick(2 * t + (ISV ? 2 : 1));
          if constexpr (ISV) free_next = t < 3 || ldu(L_CONS + ((t + 1) & 3) * 4) >= (unsigned)((t + 1) >> 2);
        }
      }
    };
    using W0 = std::integral_constant<int, WLO>; using W1 = std::integral_constant<int, WMID>; using W2 = std::integral_constant<int, WHI>;
    using KK = std::integral_constant<bool, false>; using VV = std::integral_constant<bool, true>;
    for (int t = 0; t < NTILE; ++t) {
      const unsigned slot = L_RING + (t & 3) * 32768u;
      const unsigned so = base + t * 16384u;
      if (!free_next) {
        const unsigned need = t >> 2;
        if (ldu(L_CONS + (t & 3) * 4) < need) {
          VM0();
          pub = 16 + 32 * t; st1(L_LANDED, (unsigned)pub);
          while (ldu(L_CONS + (t & 3) * 4) < need) __builtin_amdgcn_s_sleep(1);
        }
      }
      if (!PACE || mode == 1) half(W1{}, KK{}, t, slot, so); else if (mode == 0) half(W0{}, KK{}, t, slot, so); else half(W2{}, KK{}, t, slot, so);
      mode = mode_next;
      if (!PACE || mode == 1) half(W1{}, VV{}, t, slot, so); else if (mode == 0) half(W0{}, VV{}, t, slot, so); else half(W2{}, VV{}, t, slot, so);
      mode = mode_next;
    }
    vmwait<16>();
    if (pub < 32 * NTILE) st1(L_LANDED, 32u * NTILE);
    VM0();
    st1(L_LANDED, 16u + 32u * NTILE);
    STAMP(1);
    return;
  }

  if (wave == 1) {
    STAMP(2);
    const unsigned t0 = (unsigned)rt();
    const unsigned g = lane >> 4, r = lane & 15, q4 = r >> 2, p = lane & 3;
    const unsigned epoch = __hip_atomic_load((gu32*)(ws + W_EPOCH), __ATOMIC_RELAXED, __HIP_MEMORY_SCOPE_AGENT);
    const u32x4 x0 = *((const GAS u32x4*)xa + lane);
    const u32x4 x1 = *((const GAS u32x4*)xa + 64 + (lane < 48 ? lane : 47));
    const u32x4 gwv = *((const GAS u32x4*)gw + (lane < 56 ? lane : 55));
    if (lane < 16) stv(L_MISC + lane * 4, 0u);
    LGKM0();
    BAR();
    *(LAS u32x4*)(L_X + lane * 16) = x0;
    if (lane < 48) *(LAS u32x4*)(L_X + 1024 + lane * 16) = x1;
    const unsigned tag = __builtin_amdgcn_readfirstlane(epoch) + 1u;
    if (lane == 0) stv(L_TAG, tag);
    LGKM0();
    const unsigned xa_addr = opaque(L_X + (lane & 1) * 896 + g * 8);
    const unsigned wt_addr = opaque(L_R + (4 * g + q4) * 32 + p * 8);
    u32x4 xf[14];
#pragma unroll
    for (int s = 0; s < 14; ++s) { const u32x2 a0 = ld64(xa_addr + s * 64), a1 = ld64(xa_addr + s * 64 + 32); xf[s] = u32x4{a0.x, a0.y, a1.x, a1.y}; }
    float omg, gate;
    {
      const u32x4 xv = ld128(L_X + b * 896 + (lane < 56 ? lane : 55) * 16);
      float s = 0.f;
#pragma unroll
      for (int k = 0; k < 4; ++k) { s += rbf(bf2f(xv[k] & 0xffffu) * bf2f(gwv[k] & 0xffffu)); s += rbf(__uint_as_float(xv[k] & 0xffff0000u) * __uint_as_float(gwv[k] & 0xffff0000u)); }
      if (lane >= 56) s = 0.f;
      s += dppf<XOR1>(s); s += dppf<XOR2>(s); s += dppf<HMIR>(s); s += dppf<ROR8>(s); s = x32sum(x16sum(s));
      s = rbf(s);
      gate = rbf(1.f / (1.f + __expf(-s)));
      omg = rbf(1.f - gate);
    }
    auto slice_gemv = [&]() -> f32x4 {
      u32x2 bf[28];
#pragma unroll
      for (int s = 0; s < 14; ++s) { bf[2 * s] = trrd(wt_addr + s * 1024); bf[2 * s + 1] = trrd(wt_addr + s * 1024 + 512); }
      SCHED();
      f32x4 a0 = {0.f, 0.f, 0.f, 0.f}, a1 = {0.f, 0.f, 0.f, 0.f};
#pragma unroll
      for (int s = 0; s < 7; ++s) {
        a0 = mfma(xf[2 * s], u32x4{bf[4 * s].x, bf[4 * s].y, bf[4 * s + 1].x, bf[4 * s + 1].y}, a0);
        a1 = mfma(xf[2 * s + 1], u32x4{bf[4 * s + 2].x, bf[4 * s + 2].y, bf[4 * s + 3].x, bf[4 * s + 3].y}, a1);
      }
      return a0 + a1;
    };
    while (ldu(L_LANDED) < 16) __builtin_amdgcn_s_sleep(1);
    STAMP(3);
    {
      const f32x4 a = slice_gemv();
      const unsigned bits = f2bf((g & 1) ? a[1] : a[0]);
      const unsigned nb = dppu<XOR1>(bits);
      if (g < 2 && !(lane & 1)) st_gran((gu64*)(ws + W_GQ) + g * 2048 + cs * 8 + (r >> 1), tag, bits | (nb << 16));
    }
    STAMP(4);
    while ((int)((unsigned)rt() - t0) < T_Q) __builtin_amdgcn_s_sleep(4);
    {
      gu64* gq = (gu64*)(ws + W_GQ) + b * 2048 + kvh * 256 + lane;
      unsigned v[4];
      for (;;) {
        bool ok = true;
#pragma unroll
        for (int i = 0; i < 4; ++i) { const unsigned long long x = ld_gran(gq + i * 64); v[i] = (unsigned)x; ok &= (unsigned)(x >> 32) == tag; }
        if (__all(ok)) break;
        if ((unsigned)rt() - t0 > (unsigned)TMO) break;
        __builtin_amdgcn_s_sleep(8);
      }
#pragma unroll
      for (int i = 0; i < 4; ++i) *(LAS unsigned*)(L_Q + (i * 64 + lane) * 4) = v[i];
      LGKM0();
      if (lane == 0) stv(L_QRDY, 1u);
    }
    STAMP(5);
    const int fidx = kvh * 2 + (j >> 3);
    const int fs = (j & 7) * 14 + fidx;
    float yb[8];
#pragma unroll
    for (int k = 0; k < 8; ++k) yb[k] = 0.f;
    while ((int)((unsigned)rt() - t0) < T_W1) __builtin_amdgcn_s_sleep(4);
    if (fidx < 14) {
      const u32x4 r1 = mk_rsrc(w1, D * FF * 2);
      const unsigned vo = (lane >> 1) * (FF * 2) + (lane & 1) * 16;
#pragma unroll
      for (int i = 0; i < 14; ++i) dma1(L_R + i * 1024, vo, r1, fs * 32 + i * 32 * (FF * 2));
      u32x4 w2r[16];
      const GAS u32x4* w2p = (const GAS u32x4*)(w2 + (size_t)fs * 16 * D) + (lane < 56 ? lane : 55);
#pragma unroll
      for (int i = 0; i < 16; ++i) w2r[i] = __builtin_nontemporal_load(w2p + i * 56);
      VM0();
      const f32x4 a = slice_gemv();
      float hv = rbf(b ? a[1] : a[0]);
      hv = rbf(hv / (1.f + __expf(-hv)));
#pragma unroll
      for (int i = 0; i < 16; ++i) {
        const float hs = __builtin_bit_cast(float, __builtin_amdgcn_readlane(__builtin_bit_cast(int, hv), i));
#pragma unroll
        for (int k = 0; k < 4; ++k) { yb[2 * k] = fmaf(hs, bf2f(w2r[i][k] & 0xffffu), yb[2 * k]); yb[2 * k + 1] = fmaf(hs, __uint_as_float(w2r[i][k] & 0xffff0000u), yb[2 * k + 1]); }
      }
    }
    LGKM0();
    if (lane < 56) {
      *(LAS f32x4*)(L_YB + lane * 32) = f32x4{yb[0] * omg, yb[1] * omg, yb[2] * omg, yb[3] * omg};
      *(LAS f32x4*)(L_YB + lane * 32 + 16) = f32x4{yb[4] * omg, yb[5] * omg, yb[6] * omg, yb[7] * omg};
    }
    while ((int)((unsigned)rt() - t0) < T_WO) __builtin_amdgcn_s_sleep(4);
    {
      const u32x4 ro = mk_rsrc(wo, NQ * D * 2);
      const unsigned vo = lane * 16;
      const unsigned r0 = (kvh * 512 + j * 32) * (D * 2);
#pragma unroll
      for (int i = 0; i < 7; ++i) dma4(L_R + i * 4096, vo, vo, vo, vo, ro, r0 + i * 4096);
      const unsigned to = __builtin_nontemporal_load((const GAS unsigned*)out);
      asm volatile("" :: "v"(to));
    }
    VM0();
    LGKM0();
    if (lane == 0) stv(L_HRDY, 1u);
    STAMP(6);
    {
      while (ldu(L_TB) == 0) __builtin_amdgcn_s_sleep(16);
      const unsigned ta = ldu(L_TA), tb = ldu(L_TB);
      gu64* gp = (gu64*)(ws + W_GP) + grp * 32;
      if (lane < 2) st_gran(gp + j * 2 + lane, tag, lane ? tb : ta);
      while ((int)((unsigned)rt() - tb) < 250) __builtin_amdgcn_s_sleep(16);
      for (int tries = 0; tries < 12; ++tries) {
        const unsigned long long x = ld_gran(gp + (lane & 31));
        if (__all((unsigned)(x >> 32) == tag)) {
          float d = (float)(int)((unsigned)x - tb);
          d += dppf<XOR2>(d); d += dppf<ROR4>(d); d += dppf<ROR8>(d); d = x16sum(d);
          const float ma = __builtin_bit_cast(float, __builtin_amdgcn_readlane(__builtin_bit_cast(int, d), 0)) * (1.f / 16.f);
          const float mb = __builtin_bit_cast(float, __builtin_amdgcn_readlane(__builtin_bit_cast(int, d), 1)) * (1.f / 16.f);
          const float dh = (mb - ma) * (CAL_EPS / (2 * (CAL_B - CAL_A)));
          if (lane == 0) stv(L_FIN, tb + (unsigned)(int)(mb + dh * (2 * (NTILE - CAL_B))));
          LGKM0();
          if (lane == 0) stv(L_DEL, (unsigned)(dh * 16.f + 0.5f));
          break;
        }
        for (int s = 0; s < 8; ++s) __builtin_amdgcn_s_sleep(16);
      }
    }
    u32x4 wf[28];
    float ybv[7];
    {
      const unsigned wa = opaque(L_R + (8 * g + q4) * (D * 2) + p * 8);
#pragma unroll
      for (int ct = 0; ct < 28; ++ct) { const u32x2 b0 = trrd(wa + ct * 32), b1 = trrd(wa + ct * 32 + 4 * D * 2); wf[ct] = u32x4{b0.x, b0.y, b1.x, b1.y}; }
#pragma unroll
      for (int k = 0; k < 7; ++k) ybv[k] = ldf(L_YB + (64 * k + lane) * 4);
    }
    const __amdgpu_buffer_rsrc_t rs = ws_rsrc(ws);
    const int fp = wg & 127;
    unsigned o_val = opaque(W_GA + (unsigned)((grp * 16 + j) * 16 + g) * 256u + r * 16u);
    unsigned o_ml = opaque(W_ML + (unsigned)((grp * 16 + j) * 16 + ((r + g) & 15)) * 16u);
    unsigned o_bs = opaque(W_GB + (unsigned)(b * 112 * 128 + fp) * 32u + (unsigned)((((lane & 1) * 64 + (lane & ~1)) >> 2) * 4096 + (lane & 2) * 8));
    unsigned o_bl = opaque(W_GB + (unsigned)((b * 112 + (fp < 112 ? fp : 0)) * 128) * 32u + lane * 16u);
    unsigned o16w = opaque(L_O16 + r * 4), o16r = opaque(L_O16 + g * 16);
    LGKM0();
    BAR();
    STAMP(7);
    const unsigned long long t1 = rt();
    u32x4 v[4], ml;
    for (;;) {
      asm volatile("" ::: "memory");
#pragma unroll
      for (int i = 0; i < 4; ++i) v[i] = ld_gran2(rs, o_val + i * 1024);
      ml = ld_gran2(rs, o_ml);
      const unsigned bad = ((v[0].y ^ tag) | (v[0].w ^ tag) | (v[1].y ^ tag)) | ((v[1].w ^ tag) | (v[2].y ^ tag) | (v[2].w ^ tag)) |
                           ((v[3].y ^ tag) | (v[3].w ^ tag) | (ml.y ^ tag)) | (ml.w ^ tag);
      if (__all(bad == 0)) break;
      if (rt() - t1 > TMO) break;
    }
    STAMP(8);
    {
      const float Mp = __uint_as_float(ml.x), Lp = __uint_as_float(ml.z);
      float M = Mp; M = fmaxf(M, dppf<XOR1>(M)); M = fmaxf(M, dppf<XOR2>(M)); M = fmaxf(M, dppf<HMIR>(M)); M = fmaxf(M, dppf<ROR8>(M));
      const float wt = __builtin_amdgcn_exp2f(Mp - M);
      float L = wt * Lp; L += dppf<XOR1>(L); L += dppf<XOR2>(L); L += dppf<HMIR>(L); L += dppf<ROR8>(L);
      const float sc = wt * __builtin_amdgcn_rcpf(L);
      const float s0 = dppf<0x150>(sc), s1 = dppf<0x154>(sc), s2 = dppf<0x158>(sc), s3 = dppf<0x15C>(sc);
      float oa = fmaf(s1, __uint_as_float(v[1].x), s0 * __uint_as_float(v[0].x)) + fmaf(s3, __uint_as_float(v[3].x), s2 * __uint_as_float(v[2].x));
      float ob = fmaf(s1, __uint_as_float(v[1].z), s0 * __uint_as_float(v[0].z)) + fmaf(s3, __uint_as_float(v[3].z), s2 * __uint_as_float(v[2].z));
      oa = x32sum(x16sum(oa)); ob = x32sum(x16sum(ob));
      *(LAS unsigned*)o16w = cvtpk(oa, ob);
      LGKM0();
      const u32x4 af = ld128(o16r);
      float ys[8];
#pragma unroll
      for (int k = 0; k < 7; ++k) {
        float y[4];
#pragma unroll
        for (int u = 0; u < 4; ++u) y[u] = mfma(af, wf[4 * k + u], f32x4{0.f, 0.f, 0.f, 0.f})[0];
        ys[k] = fmaf(gate, g == 0 ? y[0] : g == 1 ? y[1] : g == 2 ? y[2] : y[3], ybv[k]);
      }
      ys[7] = 0.f;
#pragma unroll
      for (int k = 0; k < 8; k += 2) {
        const float an = dppf<XOR1>(ys[k]), bn = dppf<XOR1>(ys[k + 1]);
        const float v0 = (lane & 1) ? bn : ys[k], v1 = (lane & 1) ? ys[k + 1] : an;
        if (k < 6 || !(lane & 1)) st_gran2(rs, o_bs + k * 16 * 4096, tag, v0, v1);
      }
      STAMP(9);
    }
    if (fp < 112) {
      u32x4 u[4];
      for (;;) {
        asm volatile("" ::: "memory");
#pragma unroll
        for (int i = 0; i < 4; ++i) u[i] = ld_gran2(rs, o_bl + i * 1024);
        const unsigned bad = ((u[0].y ^ tag) | (u[0].w ^ tag) | (u[1].y ^ tag)) | ((u[1].w ^ tag) | (u[2].y ^ tag) | (u[2].w ^ tag)) | ((u[3].y ^ tag) | (u[3].w ^ tag));
        if (__all(bad == 0)) break;
        if (rt() - t1 > TMO) break;
      }
      float sa = (__uint_as_float(u[0].x) + __uint_as_float(u[1].x)) + (__uint_as_float(u[2].x) + __uint_as_float(u[3].x));
      float sb = (__uint_as_float(u[0].z) + __uint_as_float(u[1].z)) + (__uint_as_float(u[2].z) + __uint_as_float(u[3].z));
      sa += dppf<XOR2>(sa); sb += dppf<XOR2>(sb); sa += dppf<ROR4>(sa); sb += dppf<ROR4>(sb); sa += dppf<ROR8>(sa); sb += dppf<ROR8>(sb);
      sa = x32sum(x16sum(sa)); sb = x32sum(x16sum(sb));
      if (lane < 2) *(GAS unsigned*)(out + b * D + fp * 4 + lane * 2) = cvtpk(sa, sb);
      if (wg == 0 && lane == 0) __hip_atomic_store((gu32*)(ws + W_EPOCH), tag, __ATOMIC_RELAXED, __HIP_MEMORY_SCOPE_AGENT);
    }
    STAMP(10);
    return;
  }

  {
    const int c = wave - 2;
    const unsigned g = lane >> 4, r = lane & 15;
    BAR();
    const unsigned slot = L_RING + c * 32768u;
    unsigned ka[4], va[8];
#pragma unroll
    for (int s = 0; s < 4; ++s) ka[s] = opaque(slot + r * 512 + (((4 * s + g) ^ r) << 4));
    {
      const unsigned q4 = r >> 2, p = lane & 3, row = 4 * g + q4;
      const unsigned X = (row & 7) << 1;
#pragma unroll
      for (int n = 0; n < 8; ++n) va[n] = opaque(slot + 16384 + row * 512 + (((2 * n + (p >> 1)) ^ X) << 4) + (p & 1) * 8);
    }
    u32x4 qf[8];
    f32x4 acc[16];
    float m = -INFINITY, l = 0.f;
    {
      u32x4 kf[16]; u32x2 vf[32];
      while (ldu(L_LANDED) < 16 + 32 * c + 16) __builtin_amdgcn_s_sleep(1);
#pragma unroll
      for (int s = 0; s < 8; ++s) { kf[s] = ld128(ka[s & 3] + (s >> 2) * 256); kf[8 + s] = ld128(ka[s & 3] + (s >> 2) * 256 + 8192); }
      while (ldu(L_LANDED) < 16 + 32 * c + 32) __builtin_amdgcn_s_sleep(1);
#pragma unroll
      for (int n = 0; n < 16; ++n) { vf[2 * n] = trrd(va[n & 7] + (n >> 3) * 256); vf[2 * n + 1] = trrd(va[n & 7] + (n >> 3) * 256 + 8192); }
      LGKM0();
      if (lane == 0) stv(L_CONS + c * 4, 1u);
      while (ldu(L_QRDY) == 0) __builtin_amdgcn_s_sleep(1);
#pragma unroll
      for (int s = 0; s < 8; ++s) qf[s] = ld128(L_Q + (lane & 1) * 512 + s * 64 + g * 16);
#pragma unroll
      for (int n = 0; n < 16; ++n) acc[n] = f32x4{0.f, 0.f, 0.f, 0.f};
      const u32x4 pf = qk_softmax(kf, qf, m, l, acc);
#pragma unroll
      for (int n = 0; n < 16; ++n) { u32x4 a; a.x = vf[2 * n].x; a.y = vf[2 * n].y; a.z = vf[2 * n + 1].x; a.w = vf[2 * n + 1].y; acc[n] = mfma(a, pf, acc[n]); }
    }
    if (c == 0) STAMP(17);
    float lt = 0.f;
    for (int it = 1; it < NTILE / 4; ++it) {
      const unsigned t = c + 4 * it;
      while (ldu(L_LANDED) < 16 + 32 * t + 16) __builtin_amdgcn_s_sleep(1);
      u32x4 pf;
      {
        u32x4 kf[16];
#pragma unroll
        for (int s = 0; s < 8; ++s) { kf[s] = ld128(ka[s & 3] + (s >> 2) * 256); kf[8 + s] = ld128(ka[s & 3] + (s >> 2) * 256 + 8192); }
        pf = qk_softmax(kf, qf, m, l, acc);
      }
      lt = x32sum(x16sum(l));
      while (ldu(L_LANDED) < 16 + 32 * t + 32) __builtin_amdgcn_s_sleep(1);
      u32x2 vf[32];
#pragma unroll
      for (int n = 0; n < 16; ++n) { vf[2 * n] = trrd(va[n & 7] + (n >> 3) * 256); vf[2 * n + 1] = trrd(va[n & 7] + (n >> 3) * 256 + 8192); }
#pragma unroll
      for (int n = 0; n < 16; ++n) { u32x4 a; a.x = vf[2 * n].x; a.y = vf[2 * n].y; a.z = vf[2 * n + 1].x; a.w = vf[2 * n + 1].y; acc[n] = mfma(a, pf, acc[n]); }
      LGKM0();
      if (lane == 0) stv(L_CONS + c * 4, it + 1);
    }
    if (r < 2) {
#pragma unroll
      for (int n = 0; n < 16; ++n) *(LAS f32x4*)(slot + r * 1024 + (16 * n + 4 * g) * 4) = acc[n];
      if (g == 0) { *(LAS float*)(L_ML + (c * 2 + r) * 8) = m; *(LAS float*)(L_ML + (c * 2 + r) * 8 + 4) = lt; }
    }
    const unsigned tag = ldv(L_TAG);
    const __amdgpu_buffer_rsrc_t rs = ws_rsrc(ws);
    const int h = c >> 1, e = (c & 1) * 64 + lane;
    const unsigned o_st = W_GA + (unsigned)((grp * 16 + h * 8 + (e >> 4)) * 16 + j) * 256u + (e & 15) * 16u;
    LGKM0();
    BAR();
    {
      u32x2 ml[4], a[4];
#pragma unroll
      for (int w = 0; w < 4; ++w) { ml[w] = ld64(L_ML + (w * 2 + h) * 8); a[w] = ld64(L_RING + w * 32768 + h * 1024 + e * 8); }
      SCHED();
      const float M = fmaxf(fmaxf(__uint_as_float(ml[0].x), __uint_as_float(ml[1].x)), fmaxf(__uint_as_float(ml[2].x), __uint_as_float(ml[3].x)));
      float L = 0.f, o0 = 0.f, o1 = 0.f;
#pragma unroll
      for (int w = 0; w < 4; ++w) {
        const float wt = __builtin_amdgcn_exp2f(__uint_as_float(ml[w].x) - M);
        L = fmaf(wt, __uint_as_float(ml[w].y), L);
        o0 = fmaf(wt, __uint_as_float(a[w].x), o0); o1 = fmaf(wt, __uint_as_float(a[w].y), o1);
      }
      st_gran2(rs, o_st, tag, o0, o1);
      if (!(c & 1) && lane < 8) st_gran2(rs, W_ML + (unsigned)((grp * 16 + h * 8 + lane) * 16 + j) * 16u, tag, M, L);
      if (c == 0) STAMP(18);
    }
  }
}

extern "C" void kernel_launch(void* const* d_in, const int* in_sizes, int n_in, void* d_out, int out_size, void* d_ws, size_t ws_size, hipStream_t stream) {
  (void)in_sizes; (void)n_in; (void)out_size; (void)ws_size;
  static bool attr = false;
  if (!attr) { (void)hipFuncSetAttribute((const void*)gated_mix_kernel, hipFuncAttributeMaxDynamicSharedMemorySize, L_BYTES); attr = true; }
  gated_mix_kernel<<<NWG, NTHR, L_BYTES, stream>>>((const unsigned short*)d_in[0], (const unsigned short*)d_in[1], (const unsigned short*)d_in[2],
                                                   (const unsigned short*)d_in[3], (const unsigned short*)d_in[4], (const unsigned short*)d_in[6],
                                                   (const unsigned short*)d_in[7], (const unsigned short*)d_in[8], (unsigned short*)d_out, (unsigned char*)d_ws);
}
```

```cpp
#include <hip/hip_runtime.h>
#include <stdint.h>
#include <type_traits>

#define LAS __attribute__((address_space(3)))
#define GAS __attribute__((address_space(1)))
typedef unsigned u32x4 __attribute__((ext_vector_type(4)));
typedef unsigned u32x2 __attribute__((ext_vector_type(2)));
typedef float f32x4 __attribute__((ext_vector_type(4)));
typedef short s16x4 __attribute__((ext_vector_type(4)));
typedef __bf16 bf16x8 __attribute__((ext_vector_type(8)));
typedef GAS unsigned long long gu64;
typedef GAS unsigned gu32;

#define WLO 16
#define WMID 20
#define WHI 28
#define THR 100
#define CAL_A 14
#define CAL_B 30
#define CAL_EPS 0.995f
#define PACE 1
#define T_Q 450
#define T_W1 700
#define T_WO 1000
#define STAGGER 6
#define PSLEEP 0
#define ABL 0
#define WPOL 0
#define PREWARM 1
#define POLLFROM LD_V63
#define STAMPS 0

constexpr int D = 448, NQ = 4096, FF = 1792, SK = 32768;
constexpr int NWG = 256, NTHR = 384, NTILE = 64;
constexpr unsigned L_RING = 0;
constexpr unsigned L_R = 131072;
constexpr unsigned L_Q = 159744;
constexpr unsigned L_YB = 160768;
constexpr unsigned L_X = L_YB;
constexpr unsigned L_MISC = 162560;
constexpr unsigned L_LANDED = L_MISC, L_B1 = L_MISC + 4, L_ORDY = L_MISC + 8, L_YRDY = L_MISC + 12, L_CONS = L_MISC + 16, L_QRDY = L_MISC + 32,
                   L_TA = L_MISC + 40, L_TB = L_MISC + 44, L_FIN = L_MISC + 48, L_DEL = L_MISC + 52;
constexpr unsigned L_TAG = L_MISC + 64, L_OMG = L_MISC + 68, L_GATE = L_MISC + 72;
constexpr unsigned L_H16 = L_MISC + 96;
constexpr unsigned L_W1 = L_RING + 16384;
constexpr unsigned L_W2 = L_RING + 32768 + 16384;
constexpr unsigned LD_K63 = 16 + 32 * NTILE - 16, LD_V63 = 16 + 32 * NTILE, LD_W1 = LD_V63 + 16, LD_W2 = LD_V63 + 28, LD_ALL = 4096;
constexpr unsigned L_ML = L_MISC + 128;
constexpr unsigned L_O16 = L_MISC + 192;
constexpr unsigned L_BYTES = L_MISC + 256;
static_assert(L_BYTES <= 163840, "LDS");
constexpr size_t W_EPOCH = 0;
constexpr size_t W_GQ = 4096;
constexpr size_t W_GP = 40960;
constexpr unsigned W_ML = 65536;
constexpr unsigned W_GA = 131072;
constexpr unsigned W_GB = W_GA + (1u << 20);
constexpr unsigned W_END = W_GB + 2 * 112 * 128 * 32;
constexpr size_t W_DBG = 4u << 20;
constexpr unsigned long long TMO = 25000000ull;

#pragma clang diagnostic ignored "-Wint-to-pointer-cast"
__device__ __forceinline__ unsigned long long rt() { return __builtin_amdgcn_s_memrealtime(); }
__device__ __forceinline__ float bf2f(unsigned b) { return __uint_as_float(b << 16); }
__device__ __forceinline__ unsigned f2bf(float f) { unsigned u = __float_as_uint(f); u += 0x7FFFu + ((u >> 16) & 1u); return u >> 16; }
__device__ __forceinline__ float rbf(float f) { return bf2f(f2bf(f)); }
__device__ __forceinline__ unsigned cvtpk(float lo, float hi) { unsigned r; asm("v_cvt_pk_bf16_f32 %0, %1, %2" : "=v"(r) : "v"(lo), "v"(hi)); return r; }
template <int CTRL> __device__ __forceinline__ float dppf(float x) { return __builtin_bit_cast(float, __builtin_amdgcn_mov_dpp(__builtin_bit_cast(int, x), CTRL, 0xf, 0xf, true)); }
template <int CTRL> __device__ __forceinline__ unsigned dppu(unsigned x) { return (unsigned)__builtin_amdgcn_mov_dpp((int)x, CTRL, 0xf, 0xf, true); }
constexpr int XOR1 = 0xB1, XOR2 = 0x4E, ROR4 = 0x124, ROR8 = 0x128, HMIR = 0x141;
__device__ __forceinline__ float x16max(float x) { auto s = __builtin_amdgcn_permlane16_swap(__float_as_uint(x), __float_as_uint(x), false, false); return fmaxf(__uint_as_float(s[0]), __uint_as_float(s[1])); }
__device__ __forceinline__ float x32max(float x) { auto s = __builtin_amdgcn_permlane32_swap(__float_as_uint(x), __float_as_uint(x), false, false); return fmaxf(__uint_as_float(s[0]), __uint_as_float(s[1])); }
__device__ __forceinline__ float x16sum(float x) { auto s = __builtin_amdgcn_permlane16_swap(__float_as_uint(x), __float_as_uint(x), false, false); return __uint_as_float(s[0]) + __uint_as_float(s[1]); }
__device__ __forceinline__ float x32sum(float x) { auto s = __builtin_amdgcn_permlane32_swap(__float_as_uint(x), __float_as_uint(x), false, false); return __uint_as_float(s[0]) + __uint_as_float(s[1]); }
__device__ __forceinline__ f32x4 mfma(u32x4 a, u32x4 b, f32x4 c) { return __builtin_amdgcn_mfma_f32_16x16x32_bf16(__builtin_bit_cast(bf16x8, a), __builtin_bit_cast(bf16x8, b), c, 0, 0, 0); }
__device__ __forceinline__ u32x2 trrd(unsigned addr) { return __builtin_bit_cast(u32x2, __builtin_amdgcn_ds_read_tr16_b64_v4i16((LAS s16x4*)addr)); }
__device__ __forceinline__ u32x4 ld128(unsigned addr) { return *(const LAS u32x4*)addr; }
__device__ __forceinline__ u32x2 ld64(unsigned addr) { return *(const LAS u32x2*)addr; }
__device__ __forceinline__ float ldf(unsigned addr) { return *(const LAS float*)addr; }
__device__ __forceinline__ unsigned ldv(unsigned addr) { return *(volatile LAS unsigned*)addr; }
__device__ __forceinline__ unsigned ldu(unsigned addr) { return __builtin_amdgcn_readfirstlane(*(volatile LAS unsigned*)addr); }
__device__ __forceinline__ void stv(unsigned addr, unsigned v) { *(volatile LAS unsigned*)addr = v; }
__device__ __forceinline__ void st1(unsigned addr, unsigned v) {
  unsigned a, t;
  asm volatile("s_mov_b64 exec, 1\n\tv_mov_b32 %0, %2\n\tv_mov_b32 %1, %3\n\tds_write_b32 %0, %1\n\ts_mov_b64 exec, -1" : "=&v"(a), "=&v"(t) : "s"(addr), "s"(v) : "memory");
}
__device__ __forceinline__ float sel4(unsigned g, float a0, float a1, float a2, float a3) { const bool b0 = g & 1, b1 = g & 2; const float lo = b0 ? a1 : a0, hi = b0 ? a3 : a2; return b1 ? hi : lo; }
__device__ __forceinline__ unsigned opaque(unsigned x) { asm volatile("" : "+v"(x)); return x; }
__device__ __forceinline__ u32x4 mk_rsrc(const void* p, unsigned bytes) {
  const unsigned long long a = (unsigned long long)p;
  u32x4 r; r.x = (unsigned)a; r.y = (unsigned)(a >> 32) & 0xffffu; r.z = bytes; r.w = 0x00020000u; return r;
}
__device__ __forceinline__ void st_gran(gu64* g, unsigned tag, unsigned v) { __hip_atomic_store(g, ((unsigned long long)tag << 32) | v, __ATOMIC_RELAXED, __HIP_MEMORY_SCOPE_AGENT); }
__device__ __forceinline__ unsigned long long ld_gran(gu64* g) { return __hip_atomic_load(g, __ATOMIC_RELAXED, __HIP_MEMORY_SCOPE_AGENT); }
__device__ __forceinline__ __amdgpu_buffer_rsrc_t ws_rsrc(unsigned char* ws) { return __builtin_amdgcn_make_buffer_rsrc(ws, 0, W_END, 0x00020000); }
__device__ __forceinline__ void st_gran2(__amdgpu_buffer_rsrc_t rs, unsigned off, unsigned tag, float v0, float v1) {
  __builtin_amdgcn_raw_buffer_store_b128(u32x4{__float_as_uint(v0), tag, __float_as_uint(v1), tag}, rs, off, 0, 16);
}
__device__ __forceinline__ u32x4 ld_gran2(__amdgpu_buffer_rsrc_t rs, unsigned off) { return __builtin_amdgcn_raw_buffer_load_b128(rs, off, 0, 16); }
#define LGKM0() asm volatile("s_waitcnt lgkmcnt(0)" ::: "memory")
#define VM0() asm volatile("s_waitcnt vmcnt(0)" ::: "memory")
#define BAR() asm volatile("s_barrier" ::: "memory")
#define WAIT_GE(a, v) do { while (ldu(a) < (unsigned)(v)) __builtin_amdgcn_s_sleep(1); asm volatile("" ::: "memory"); } while (0)
#define SPIN_GE(a, v) do { while (ldu(a) < (unsigned)(v)) { } asm volatile("" ::: "memory"); } while (0)
#define SCHED() __builtin_amdgcn_sched_barrier(0)
template <int N> __device__ __forceinline__ void vmwait() { asm volatile("s_waitcnt vmcnt(%0)" :: "i"(N) : "memory"); }

__device__ __forceinline__ void dma4(unsigned m0v, unsigned v0, unsigned v1, unsigned v2, unsigned v3, u32x4 rs, unsigned soff) {
  asm volatile("s_mov_b32 m0, %0\n\ts_nop 0\n\t"
               "buffer_load_dwordx4 %1, %5, %6 offen nt lds\n\t"
               "buffer_load_dwordx4 %2, %5, %6 offen offset:1024 nt lds\n\t"
               "buffer_load_dwordx4 %3, %5, %6 offen offset:2048 nt lds\n\t"
               "buffer_load_dwordx4 %4, %5, %6 offen offset:3072 nt lds"
               :: "s"(m0v), "v"(v0), "v"(v1), "v"(v2), "v"(v3), "s"(rs), "s"(soff) : "memory");
}
__device__ __forceinline__ void dma4w(unsigned m0v, unsigned v0, u32x4 rs, unsigned soff) {
#define WPOLS "nt "
  asm volatile("s_mov_b32 m0, %0\n\ts_nop 0\n\t"
               "buffer_load_dwordx4 %1, %2, %3 offen " WPOLS "lds\n\t"
               "buffer_load_dwordx4 %1, %2, %3 offen offset:1024 " WPOLS "lds\n\t"
               "buffer_load_dwordx4 %1, %2, %3 offen offset:2048 " WPOLS "lds\n\t"
               "buffer_load_dwordx4 %1, %2, %3 offen offset:3072 " WPOLS "lds"
               :: "s"(m0v), "v"(v0), "s"(rs), "s"(soff) : "memory");
}
__device__ __forceinline__ void dma1w(unsigned m0v, unsigned v0, u32x4 rs, unsigned soff) {
  asm volatile("s_mov_b32 m0, %0\n\ts_nop 0\n\tbuffer_load_dwordx4 %1, %2, %3 offen " WPOLS "lds" :: "s"(m0v), "v"(v0), "s"(rs), "s"(soff) : "memory");
}
__device__ __forceinline__ void dma1(unsigned m0v, unsigned v0, u32x4 rs, unsigned soff) {
  asm volatile("s_mov_b32 m0, %0\n\ts_nop 0\n\tbuffer_load_dwordx4 %1, %2, %3 offen nt lds" :: "s"(m0v), "v"(v0), "s"(rs), "s"(soff) : "memory");
}

#define STAMP(i) do { } while (0)

__device__ __forceinline__ u32x4 qk_softmax(const u32x4 (&kf)[16], const u32x4 (&qf)[8], float& m, float& l, f32x4 (&acc)[16]) {
  constexpr float C2 = 0.0625f * 1.4426950408889634f;
  f32x4 s0 = {0.f, 0.f, 0.f, 0.f}, s1 = {0.f, 0.f, 0.f, 0.f};
#pragma unroll
  for (int s = 0; s < 8; ++s) { s0 = mfma(kf[s], qf[s], s0); s1 = mfma(kf[8 + s], qf[s], s1); }
  float tm = fmaxf(fmaxf(fmaxf(s0[0], s0[1]), fmaxf(s0[2], s0[3])), fmaxf(fmaxf(s1[0], s1[1]), fmaxf(s1[2], s1[3])));
  tm = x32max(x16max(tm)) * C2;
  if (__any(tm > m)) {
    const float mn = fmaxf(m, tm);
    const float alpha = __builtin_amdgcn_exp2f(m - mn);
    m = mn; l *= alpha;
    const float al0 = dppf<0x00>(alpha), al1 = dppf<0x55>(alpha);
#pragma unroll
    for (int n = 0; n < 16; ++n) { acc[n][0] *= al0; acc[n][1] *= al1; }
  }
  float p[8];
#pragma unroll
  for (int i = 0; i < 4; ++i) { p[i] = __builtin_amdgcn_exp2f(fmaf(s0[i], C2, -m)); p[4 + i] = __builtin_amdgcn_exp2f(fmaf(s1[i], C2, -m)); }
  l += ((p[0] + p[1]) + (p[2] + p[3])) + ((p[4] + p[5]) + (p[6] + p[7]));
  u32x4 pf; pf.x = cvtpk(p[0], p[1]); pf.y = cvtpk(p[2], p[3]); pf.z = cvtpk(p[4], p[5]); pf.w = cvtpk(p[6], p[7]);
  return pf;
}

template <int K0, bool LAST>
__device__ __forceinline__ void project(int lane, unsigned tag, float gate, __amdgpu_buffer_rsrc_t rs, unsigned o_bs) {
  constexpr int NK = LAST ? 3 : 2;
  const unsigned g = lane >> 4, r = lane & 15, q4 = r >> 2, p = lane & 3;
  const unsigned wa = opaque(L_R + (8 * g + q4) * (D * 2) + p * 8), o16r = opaque(L_O16 + g * 16);
  WAIT_GE(L_LANDED, LD_ALL);
  u32x4 wf[4 * NK];
  float ybv[NK];
#pragma unroll
  for (int kk = 0; kk < NK; ++kk)
#pragma unroll
    for (int u = 0; u < 4; ++u) {
      const int ct = 4 * (kk < 2 ? K0 + kk : 6) + u;
      const u32x2 b0 = trrd(wa + ct * 32), b1 = trrd(wa + ct * 32 + 4 * D * 2);
      wf[4 * kk + u] = u32x4{b0.x, b0.y, b1.x, b1.y};
    }
  WAIT_GE(L_YRDY, 1u);
#pragma unroll
  for (int kk = 0; kk < NK; ++kk) ybv[kk] = ldf(L_YB + (64 * (kk < 2 ? K0 + kk : 6) + lane) * 4);
  LGKM0();
  SPIN_GE(L_ORDY, 1u);
  const u32x4 af = ld128(o16r);
  float ys[NK];
#pragma unroll
  for (int kk = 0; kk < NK; ++kk) {
    float y[4];
#pragma unroll
    for (int u = 0; u < 4; ++u) y[u] = mfma(af, wf[4 * kk + u], f32x4{0.f, 0.f, 0.f, 0.f})[0];
    ys[kk] = fmaf(gate, sel4(g, y[0], y[1], y[2], y[3]), ybv[kk]);
  }
  const float an = dppf<XOR1>(ys[0]), bn = dppf<XOR1>(ys[1]);
  st_gran2(rs, o_bs + K0 * 16 * 4096, tag, (lane & 1) ? bn : ys[0], (lane & 1) ? ys[1] : an);
  if (LAST) {
    const float cn = dppf<XOR1>(ys[NK - 1]);
    if (!(lane & 1)) st_gran2(rs, o_bs + 6 * 16 * 4096, tag, ys[NK - 1], cn);
  }
}
__device__ __forceinline__ unsigned gb_store_off(int b, int fp, int lane) {
  return W_GB + (unsigned)(b * 112 * 128 + fp) * 32u + (unsigned)((((lane & 1) * 64 + (lane & ~1)) >> 2) * 4096 + (lane & 2) * 8);
}

__global__ void __launch_bounds__(NTHR, 2)
gated_mix_kernel(const unsigned short* __restrict__ xa, const unsigned short* __restrict__ kc, const unsigned short* __restrict__ vc,
                 const unsigned short* __restrict__ wq, const unsigned short* __restrict__ wo, const unsigned short* __restrict__ w1,
                 const unsigned short* __restrict__ w2, const unsigned short* __restrict__ gw, unsigned short* __restrict__ out,
                 unsigned char* __restrict__ ws) {
  const int tid = threadIdx.x, lane = tid & 63;
  const int wave = __builtin_amdgcn_readfirstlane(tid >> 6);
  const int wg = blockIdx.x;
  const int grp = wg >> 4, j = wg & 15, b = grp >> 3, kvh = grp & 7;
  const unsigned cs = (wg & 7) * 32 + (wg >> 3);

  if (wave == 0) {
    STAMP(0);
    __builtin_amdgcn_s_setprio(3);
    {
      const u32x4 rq = mk_rsrc(wq, D * NQ * 2);
      const unsigned vo = (lane >> 1) * (NQ * 2) + (lane & 1) * 16;
#pragma unroll
      for (int i = 0; i < 14; ++i) dma1(L_R + i * 1024, vo, rq, cs * 32 + i * 32 * (NQ * 2));
    }
    const u32x4 rk = mk_rsrc(kc, 256u << 20), rv = mk_rsrc(vc, 256u << 20);
    unsigned vk[8], vv[4];
    const unsigned hi = lane >> 5, pc = lane & 31;
#pragma unroll
    for (int i = 0; i < 8; ++i) vk[i] = hi * 512 + ((pc ^ (2 * i + hi)) << 4);
#pragma unroll
    for (int i = 0; i < 4; ++i) vv[i] = hi * 512 + ((pc ^ ((2 * i + hi) << 1)) << 4);
    const unsigned base = (unsigned)(grp * SK + j * 2048) * 512u;
    int pub = 0;
    unsigned del = 0, fin = 0;
    int mode = 1, mode_next = 1;
    bool free_next = true;
    auto pick = [&](int u) {
      if (!PACE) return;
      if (u <= 2 * CAL_B + 1) {
        if (u == 2 * CAL_A + 1) st1(L_TA, (unsigned)rt() | 1u);
        if (u == 2 * CAL_B + 1) st1(L_TB, (unsigned)rt() | 1u);
        return;
      }
      if (!del) { del = ldu(L_DEL); fin = ldu(L_FIN); if (!del) return; }
      const int lag = (int)((unsigned)rt() - fin) + (int)(((2 * NTILE + 1 - u) * del) >> 4);
      mode_next = lag < -THR ? 0 : lag > THR ? 2 : 1;
    };
    auto half = [&](auto wc, auto vc_, int t, unsigned slot, unsigned so) {
      constexpr int W = decltype(wc)::value; constexpr bool ISV = decltype(vc_)::value;
#pragma unroll
      for (int gi = 0; gi < 4; ++gi) {
        vmwait<W - 4>();
        if constexpr (ISV) dma4(slot + 16384 + gi * 4096, vv[0], vv[1], vv[2], vv[3], rv, so + gi * 4096);
        else if (gi & 1) dma4(slot + gi * 4096, vk[4], vk[5], vk[6], vk[7], rk, so + gi * 4096);
        else dma4(slot + gi * 4096, vk[0], vk[1], vk[2], vk[3], rk, so + gi * 4096);
        if (((4 * gi - (W - 4)) & 15) == 0) {
          const int cand = 16 + 32 * t + (ISV ? 16 : 0) + 4 * gi - (W - 4);
          if (cand > pub) { pub = cand; st1(L_LANDED, (unsigned)cand); }
        }
        if (gi == 0) {
          if constexpr (!ISV) { if (t == 0) BAR(); }
          pick(2 * t + (ISV ? 2 : 1));
          if constexpr (ISV) free_next = t < 3 || ldu(L_CONS + ((t + 1) & 3) * 4) >= (unsigned)((t + 1) >> 2);
        }
      }
    };
    using W0 = std::integral_constant<int, WLO>; using W1 = std::integral_constant<int, WMID>; using W2 = std::integral_constant<int, WHI>;
    using KK = std::integral_constant<bool, false>; using VV = std::integral_constant<bool, true>;
    for (int t = 0; t < NTILE; ++t) {
      const unsigned slot = L_RING + (t & 3) * 32768u;
      const unsigned so = base + t * 16384u;
      if (!free_next) {
        const unsigned need = t >> 2;
        if (ldu(L_CONS + (t & 3) * 4) < need) {
          VM0();
          pub = 16 + 32 * t; st1(L_LANDED, (unsigned)pub);
          WAIT_GE(L_CONS + (t & 3) * 4, need);
        }
      }
      if (!PACE || mode == 1) half(W1{}, KK{}, t, slot, so); else if (mode == 0) half(W0{}, KK{}, t, slot, so); else half(W2{}, KK{}, t, slot, so);
      mode = mode_next;
      if (!PACE || mode == 1) half(W1{}, VV{}, t, slot, so); else if (mode == 0) half(W0{}, VV{}, t, slot, so); else half(W2{}, VV{}, t, slot, so);
      mode = mode_next;
    }
    {
      const int fidx = kvh * 2 + (j >> 3), fs = (j & 7) * 14 + fidx;
      const unsigned keep = ((ABL & 4) && b == 1) ? 0u : 1u, keepo = ((ABL & 8) && b == 1) ? 0u : keep;
      const u32x4 r1 = mk_rsrc(w1, D * FF * 2 * keep), r2 = mk_rsrc(w2, FF * D * 2 * keep), ro = mk_rsrc(wo, NQ * D * 2 * keepo);
      const unsigned vo1 = (lane >> 1) * (FF * 2) + (lane & 1) * 16, vol = lane * 16;
      const unsigned so1 = fs * 32, so2 = fs * 16 * (D * 2), soo = (kvh * 512 + j * 32) * (D * 2);
      auto pubx = [&](int cand) { if (cand > pub) { pub = cand; st1(L_LANDED, (unsigned)cand); } };
      vmwait<16>(); pubx(LD_K63);
      if (fidx < 14) {
        WAIT_GE(L_CONS, NTILE / 4); WAIT_GE(L_CONS + 4, NTILE / 4);
#pragma unroll
        for (int e = 0; e < 14; ++e) dma1(L_W1 + e * 1024, vo1, r1, so1 + e * 32 * (FF * 2));
#pragma unroll
        for (int e = 0; e < 3; ++e) dma4(L_W2 + e * 4096, vol, vol, vol, vol, r2, so2 + e * 4096);
        dma1(L_W2 + 12 * 1024, vol, r2, so2 + 12 * 1024); dma1(L_W2 + 13 * 1024, vol, r2, so2 + 13 * 1024);
        vmwait<28>(); pubx(LD_V63); STAMP(1);
#pragma unroll
        for (int e = 0; e < 7; ++e) dma4(L_R + e * 4096, vol, vol, vol, vol, ro, soo + e * 4096);
        vmwait<42>(); pubx(LD_W1);
        vmwait<28>(); pubx(LD_W2);
      } else {
#pragma unroll
        for (int e = 0; e < 7; ++e) dma4(L_R + e * 4096, vol, vol, vol, vol, ro, soo + e * 4096);
        vmwait<28>(); pubx(LD_V63); STAMP(1);
      }
      VM0();
      st1(L_LANDED, LD_ALL);
    }
    STAMP(23);
    return;
  }

  if (wave == 1) {
    STAMP(2);
    const unsigned t0 = (unsigned)rt();
    const unsigned g = lane >> 4, r = lane & 15, q4 = r >> 2, p = lane & 3;
    const unsigned epoch = __hip_atomic_load((gu32*)(ws + W_EPOCH), __ATOMIC_RELAXED, __HIP_MEMORY_SCOPE_AGENT);
    const u32x4 x0 = *((const GAS u32x4*)xa + lane);
    const u32x4 x1 = *((const GAS u32x4*)xa + 64 + (lane < 48 ? lane : 47));
    const u32x4 gwv = *((const GAS u32x4*)gw + (lane < 56 ? lane : 55));
    u32x4 pw[9];
    if (PREWARM) {
      const __amdgpu_buffer_rsrc_t rs0 = ws_rsrc(ws);
      const unsigned o_val = W_GA + (unsigned)((grp * 16 + j) * 16 + g) * 256u + r * 16u;
      const unsigned o_ml = W_ML + (unsigned)((grp * 16 + j) * 16 + r) * 16u;
      const unsigned o_bl = W_GB + (unsigned)((b * 112 + ((wg & 127) < 112 ? (wg & 127) : 0)) * 128) * 32u + lane * 16u;
      if (PREWARM == 3) {
        const unsigned off = lane < 14 ? W_GB + b * (448u << 10) * 0u + lane * 65536u : lane == 14 ? W_GA + grp * 65536u : W_ML;
        const unsigned x = __builtin_amdgcn_raw_buffer_load_b32(rs0, off, 0, 16);
#pragma unroll
        for (int i = 0; i < 9; ++i) pw[i] = u32x4{x, x, x, x};
      } else if (PREWARM == 4) {
#pragma unroll
        for (int i = 0; i < 4; ++i) { pw[i] = ld_gran2(rs0, o_val + i * 1024); pw[5 + i] = pw[i]; }
        pw[4] = ld_gran2(rs0, o_ml);
      } else {
#pragma unroll
      for (int i = 0; i < 4; ++i) { pw[i] = PREWARM == 1 ? ld_gran2(rs0, o_val + i * 1024) : __builtin_amdgcn_raw_buffer_load_b128(rs0, o_val + i * 1024, 0, 0); pw[5 + i] = PREWARM == 1 ? ld_gran2(rs0, o_bl + i * 1024) : __builtin_amdgcn_raw_buffer_load_b128(rs0, o_bl + i * 1024, 0, 0); }
      pw[4] = PREWARM == 1 ? ld_gran2(rs0, o_ml) : __builtin_amdgcn_raw_buffer_load_b128(rs0, o_ml, 0, 0);
      }
    }
    if (lane < 16) stv(L_MISC + lane * 4, 0u);
    LGKM0();
    BAR();
    *(LAS u32x4*)(L_X + lane * 16) = x0;
    if (lane < 48) *(LAS u32x4*)(L_X + 1024 + lane * 16) = x1;
    const unsigned tag = __builtin_amdgcn_readfirstlane(epoch) + 1u;
    if (lane == 0) stv(L_TAG, tag);
    LGKM0();
    const unsigned xa_addr = opaque(L_X + (lane & 1) * 896 + g * 8);
    const unsigned wt_addr = opaque(L_R + (4 * g + q4) * 32 + p * 8);
    u32x4 xf[14];
#pragma unroll
    for (int s = 0; s < 14; ++s) { const u32x2 a0 = ld64(xa_addr + s * 64), a1 = ld64(xa_addr + s * 64 + 32); xf[s] = u32x4{a0.x, a0.y, a1.x, a1.y}; }
    float gate;
    {
      const u32x4 xv = ld128(L_X + b * 896 + (lane < 56 ? lane : 55) * 16);
      float s = 0.f;
#pragma unroll
      for (int k = 0; k < 4; ++k) { s += rbf(bf2f(xv[k] & 0xffffu) * bf2f(gwv[k] & 0xffffu)); s += rbf(__uint_as_float(xv[k] & 0xffff0000u) * __uint_as_float(gwv[k] & 0xffff0000u)); }
      if (lane >= 56) s = 0.f;
      s += dppf<XOR1>(s); s += dppf<XOR2>(s); s += dppf<HMIR>(s); s += dppf<ROR8>(s); s = x32sum(x16sum(s));
      s = rbf(s);
      gate = rbf(1.f / (1.f + __expf(-s)));
      if (lane == 0) { stv(L_OMG, __float_as_uint(rbf(1.f - gate))); stv(L_GATE, __float_as_uint(gate)); }
    }
    auto slice_gemv = [&]() -> f32x4 {
      u32x2 bf[28];
#pragma unroll
      for (int s = 0; s < 14; ++s) { bf[2 * s] = trrd(wt_addr + s * 1024); bf[2 * s + 1] = trrd(wt_addr + s * 1024 + 512); }
      SCHED();
      f32x4 a0 = {0.f, 0.f, 0.f, 0.f}, a1 = {0.f, 0.f, 0.f, 0.f};
#pragma unroll
      for (int s = 0; s < 7; ++s) {
        a0 = mfma(xf[2 * s], u32x4{bf[4 * s].x, bf[4 * s].y, bf[4 * s + 1].x, bf[4 * s + 1].y}, a0);
        a1 = mfma(xf[2 * s + 1], u32x4{bf[4 * s + 2].x, bf[4 * s + 2].y, bf[4 * s + 3].x, bf[4 * s + 3].y}, a1);
      }
      return a0 + a1;
    };
    WAIT_GE(L_LANDED, 16);
    STAMP(3);
    {
      const f32x4 a = slice_gemv();
      const unsigned bits = f2bf((g & 1) ? a[1] : a[0]);
      const unsigned nb = dppu<XOR1>(bits);
      if (g < 2 && !(lane & 1)) st_gran((gu64*)(ws + W_GQ) + g * 2048 + cs * 8 + (r >> 1), tag, bits | (nb << 16));
    }
    STAMP(4);
    if (PREWARM) {
#pragma unroll
      for (int i = 0; i < 9; ++i) asm volatile("" :: "v"(pw[i]));
    }
    while ((int)((unsigned)rt() - t0) < T_Q) __builtin_amdgcn_s_sleep(4);
    {
      gu64* gq = (gu64*)(ws + W_GQ) + b * 2048 + kvh * 256 + lane;
      unsigned v[4];
      for (;;) {
        bool ok = true;
#pragma unroll
        for (int i = 0; i < 4; ++i) { const unsigned long long x = ld_gran(gq + i * 64); v[i] = (unsigned)x; ok &= (unsigned)(x >> 32) == tag; }
        if (__all(ok)) break;
        if ((unsigned)rt() - t0 > (unsigned)TMO) break;
        __builtin_amdgcn_s_sleep(8);
      }
#pragma unroll
      for (int i = 0; i < 4; ++i) *(LAS unsigned*)(L_Q + (i * 64 + lane) * 4) = v[i];
      LGKM0();
      if (lane == 0) stv(L_QRDY, 1u);
    }
    STAMP(5);
    { const unsigned to = __builtin_nontemporal_load((const GAS unsigned*)out); asm volatile("" :: "v"(to)); }
    {
      while (ldu(L_TB) == 0) __builtin_amdgcn_s_sleep(16);
      const unsigned ta = ldu(L_TA), tb = ldu(L_TB);
      gu64* gp = (gu64*)(ws + W_GP) + grp * 32;
      if (lane < 2) st_gran(gp + j * 2 + lane, tag, lane ? tb : ta);
      while ((int)((unsigned)rt() - tb) < 250) __builtin_amdgcn_s_sleep(16);
      for (int tries = 0; tries < 12; ++tries) {
        const unsigned long long x = ld_gran(gp + (lane & 31));
        if (__all((unsigned)(x >> 32) == tag)) {
          float d = (float)(int)((unsigned)x - tb);
          d += dppf<XOR2>(d); d += dppf<ROR4>(d); d += dppf<ROR8>(d); d = x16sum(d);
          const float ma = __builtin_bit_cast(float, __builtin_amdgcn_readlane(__builtin_bit_cast(int, d), 0)) * (1.f / 16.f);
          const float mb = __builtin_bit_cast(float, __builtin_amdgcn_readlane(__builtin_bit_cast(int, d), 1)) * (1.f / 16.f);
          const float dh = (mb - ma) * (CAL_EPS / (2 * (CAL_B - CAL_A)));
          if (lane == 0) stv(L_FIN, tb + (unsigned)(int)(mb + dh * (2 * (NTILE - CAL_B))));
          LGKM0();
          if (lane == 0) stv(L_DEL, (unsigned)(dh * 16.f + 0.5f));
          break;
        }
        for (int s = 0; s < 8; ++s) __builtin_amdgcn_s_sleep(16);
      }
    }
    const __amdgpu_buffer_rsrc_t rs = ws_rsrc(ws);
    const int fp = wg & 127;
    unsigned o_bs = opaque(W_GB + (unsigned)(b * 112 * 128 + fp) * 32u + (unsigned)((((lane & 1) * 64 + (lane & ~1)) >> 2) * 4096 + (lane & 2) * 8));
    unsigned o_bl = opaque(W_GB + (unsigned)((b * 112 + (fp < 112 ? fp : 0)) * 128) * 32u + lane * 16u);
    project<0, true>(lane, tag, gate, rs, o_bs);
    STAMP(9);
    const unsigned long long t1 = rt();
    if (fp < 112) {
      u32x4 u[4];
      unsigned spins = 0;
      for (;;) {
        asm volatile("" ::: "memory");
#pragma unroll
        for (int i = 0; i < 4; ++i) u[i] = ld_gran2(rs, o_bl + i * 1024);
        const unsigned bad = ((u[0].y ^ tag) | (u[0].w ^ tag) | (u[1].y ^ tag)) | ((u[1].w ^ tag) | (u[2].y ^ tag) | (u[2].w ^ tag)) | ((u[3].y ^ tag) | (u[3].w ^ tag));
        if (__all(bad == 0)) break;
        if (rt() - t1 > TMO) break;
      }
      float sa = (__uint_as_float(u[0].x) + __uint_as_float(u[1].x)) + (__uint_as_float(u[2].x) + __uint_as_float(u[3].x));
      float sb = (__uint_as_float(u[0].z) + __uint_as_float(u[1].z)) + (__uint_as_float(u[2].z) + __uint_as_float(u[3].z));
      sa += dppf<XOR2>(sa); sb += dppf<XOR2>(sb); sa += dppf<ROR4>(sa); sb += dppf<ROR4>(sb); sa += dppf<ROR8>(sa); sb += dppf<ROR8>(sb);
      sa = x32sum(x16sum(sa)); sb = x32sum(x16sum(sb));
      if (lane < 2) *(GAS unsigned*)(out + b * D + fp * 4 + lane * 2) = cvtpk(sa, sb);
      if (wg == 0 && lane == 0) __hip_atomic_store((gu32*)(ws + W_EPOCH), tag, __ATOMIC_RELAXED, __HIP_MEMORY_SCOPE_AGENT);
    }
    STAMP(10);
    return;
  }

  {
    const int c = wave - 2;
    const unsigned g = lane >> 4, r = lane & 15;
    BAR();
    const unsigned slot = L_RING + c * 32768u;
    unsigned ka[4], va[8];
#pragma unroll
    for (int s = 0; s < 4; ++s) ka[s] = opaque(slot + r * 512 + (((4 * s + g) ^ r) << 4));
    {
      const unsigned q4 = r >> 2, p = lane & 3, row = 4 * g + q4;
      const unsigned X = (row & 7) << 1;
#pragma unroll
      for (int n = 0; n < 8; ++n) va[n] = opaque(slot + 16384 + row * 512 + (((2 * n + (p >> 1)) ^ X) << 4) + (p & 1) * 8);
    }
    u32x4 qf[8];
    f32x4 acc[16];
    float m = -INFINITY, l = 0.f;
    {
      u32x4 kf[16]; u32x2 vf[32];
      WAIT_GE(L_LANDED, 16 + 32 * c + 16);
#pragma unroll
      for (int s = 0; s < 8; ++s) { kf[s] = ld128(ka[s & 3] + (s >> 2) * 256); kf[8 + s] = ld128(ka[s & 3] + (s >> 2) * 256 + 8192); }
      WAIT_GE(L_LANDED, 16 + 32 * c + 32);
#pragma unroll
      for (int n = 0; n < 16; ++n) { vf[2 * n] = trrd(va[n & 7] + (n >> 3) * 256); vf[2 * n + 1] = trrd(va[n & 7] + (n >> 3) * 256 + 8192); }
      LGKM0();
      if (lane == 0) stv(L_CONS + c * 4, 1u);
      WAIT_GE(L_QRDY, 1u);
#pragma unroll
      for (int s = 0; s < 8; ++s) qf[s] = ld128(L_Q + (lane & 1) * 512 + s * 64 + g * 16);
#pragma unroll
      for (int n = 0; n < 16; ++n) acc[n] = f32x4{0.f, 0.f, 0.f, 0.f};
      const u32x4 pf = qk_softmax(kf, qf, m, l, acc);
#pragma unroll
      for (int n = 0; n < 16; ++n) { u32x4 a; a.x = vf[2 * n].x; a.y = vf[2 * n].y; a.z = vf[2 * n + 1].x; a.w = vf[2 * n + 1].y; acc[n] = mfma(pf, a, acc[n]); }
    }
    if (c == 0) STAMP(17);
    const unsigned tag = ldv(L_TAG);
    const __amdgpu_buffer_rsrc_t rs = ws_rsrc(ws);
    float lt = 0.f;
    float pre[4][2], wt3[2] = {0.f, 0.f}, Mf[2] = {0.f, 0.f}, Lf[2] = {0.f, 0.f};
#pragma unroll
    for (int i = 0; i < 4; ++i) { pre[i][0] = 0.f; pre[i][1] = 0.f; }
    const unsigned pa = opaque((64 * g + r) * 4);
    for (int it = 1; it < NTILE / 4; ++it) {
      const unsigned t = c + 4 * it;
      WAIT_GE(L_LANDED, 16 + 32 * t + 16);
      u32x4 pf;
      {
        u32x4 kf[16];
#pragma unroll
        for (int s = 0; s < 8; ++s) { kf[s] = ld128(ka[s & 3] + (s >> 2) * 256); kf[8 + s] = ld128(ka[s & 3] + (s >> 2) * 256 + 8192); }
        pf = qk_softmax(kf, qf, m, l, acc);
      }
      lt = x32sum(x16sum(l));
      if (t == NTILE - 1) {
        WAIT_GE(L_B1, 3u);
        const float m3[2] = {dppf<0x00>(m), dppf<0x55>(m)}, l3[2] = {dppf<0x00>(lt), dppf<0x55>(lt)};
        f32x4 ml[3]; float a[3][4][2];
#pragma unroll
        for (int w = 0; w < 3; ++w) {
          ml[w] = *(const LAS f32x4*)(L_ML + w * 16);
#pragma unroll
          for (int i = 0; i < 4; ++i) { a[w][i][0] = ldf(L_RING + w * 32768 + pa + i * 64); a[w][i][1] = ldf(L_RING + w * 32768 + 1024 + pa + i * 64); }
        }
#pragma unroll
        for (int h = 0; h < 2; ++h) {
          const float M = fmaxf(fmaxf(ml[0][2 * h], ml[1][2 * h]), fmaxf(ml[2][2 * h], m3[h]));
          wt3[h] = __builtin_amdgcn_exp2f(m3[h] - M);
          float L = wt3[h] * l3[h];
#pragma unroll
          for (int w = 0; w < 3; ++w) {
            const float wt = __builtin_amdgcn_exp2f(ml[w][2 * h] - M);
            L = fmaf(wt, ml[w][2 * h + 1], L);
#pragma unroll
            for (int i = 0; i < 4; ++i) pre[i][h] = fmaf(wt, a[w][i][h], pre[i][h]);
          }
          Mf[h] = M; Lf[h] = L;
        }
        asm volatile("" : "+v"(pre[0][0]), "+v"(pre[0][1]), "+v"(pre[1][0]), "+v"(pre[1][1]), "+v"(pre[2][0]), "+v"(pre[2][1]), "+v"(pre[3][0]), "+v"(pre[3][1]),
                          "+v"(wt3[0]), "+v"(wt3[1]), "+v"(Mf[0]), "+v"(Mf[1]), "+v"(Lf[0]), "+v"(Lf[1]));
        STAMP(25);
        SPIN_GE(L_LANDED, 16 + 32 * t + 32);
        STAMP(26);
      } else {
        WAIT_GE(L_LANDED, 16 + 32 * t + 32);
      }
      u32x2 vf[32];
#pragma unroll
      for (int n = 0; n < 16; ++n) { vf[2 * n] = trrd(va[n & 7] + (n >> 3) * 256); vf[2 * n + 1] = trrd(va[n & 7] + (n >> 3) * 256 + 8192); }
#pragma unroll
      for (int n = 0; n < 16; ++n) { u32x4 a; a.x = vf[2 * n].x; a.y = vf[2 * n].y; a.z = vf[2 * n + 1].x; a.w = vf[2 * n + 1].y; acc[n] = mfma(pf, a, acc[n]); }
      LGKM0();
      if (lane == 0) stv(L_CONS + c * 4, it + 1);
    }
    float sel[4][2];
#pragma unroll
    for (int i = 0; i < 4; ++i)
#pragma unroll
      for (int h = 0; h < 2; ++h) sel[i][h] = sel4(g, acc[i][h], acc[4 + i][h], acc[8 + i][h], acc[12 + i][h]);
    if (c < 3) {
#pragma unroll
      for (int i = 0; i < 4; ++i) { *(LAS float*)(slot + pa + i * 64) = sel[i][0]; *(LAS float*)(slot + 1024 + pa + i * 64) = sel[i][1]; }
      if (lane < 2) { *(LAS float*)(L_ML + (c * 2 + lane) * 8) = m; *(LAS float*)(L_ML + (c * 2 + lane) * 8 + 4) = lt; }
      LGKM0();
      if (lane == 0) (void)__hip_atomic_fetch_add((LAS unsigned*)L_B1, 1u, __ATOMIC_RELAXED, __HIP_MEMORY_SCOPE_WORKGROUP);
    } else {
      const unsigned h = lane & 1;
      const unsigned o_st = W_GA + (unsigned)((grp * 16 + h * 8 + 2 * g) * 16 + j) * 256u + (r >> 1) * 16u;
#pragma unroll
      for (int i = 0; i < 4; ++i) {
        const float v0 = fmaf(wt3[0], sel[i][0], pre[i][0]), v1 = fmaf(wt3[1], sel[i][1], pre[i][1]);
        const float n0 = dppf<XOR1>(v0), n1 = dppf<XOR1>(v1);
        st_gran2(rs, o_st + (i >> 1) * 4096 + (i & 1) * 128, tag, h ? n1 : v0, h ? v1 : n0);
      }
      if (lane < 16) st_gran2(rs, W_ML + (unsigned)((grp * 16 + lane) * 16 + j) * 16u, tag, lane < 8 ? Mf[0] : Mf[1], lane < 8 ? Lf[0] : Lf[1]);
      STAMP(18);
    }
    if (c == 0) {
      const unsigned o_val = opaque(W_GA + (unsigned)((grp * 16 + j) * 16 + g) * 256u + r * 16u);
      const unsigned o_ml = opaque(W_ML + (unsigned)((grp * 16 + j) * 16 + ((r + g) & 15)) * 16u);
      WAIT_GE(L_LANDED, POLLFROM);
      const unsigned long long t1 = rt();
      unsigned spins = 0;
      u32x4 v[4], ml;
      for (;;) {
        asm volatile("" ::: "memory");
#pragma unroll
        for (int i = 0; i < 4; ++i) v[i] = ld_gran2(rs, o_val + i * 1024);
        ml = ld_gran2(rs, o_ml);
        const unsigned bad = ((v[0].y ^ tag) | (v[0].w ^ tag) | (v[1].y ^ tag)) | ((v[1].w ^ tag) | (v[2].y ^ tag) | (v[2].w ^ tag)) |
                             ((v[3].y ^ tag) | (v[3].w ^ tag) | (ml.y ^ tag)) | (ml.w ^ tag);
        if (__all(bad == 0)) break;
        if ((++spins & 63u) == 0 && rt() - t1 > TMO) break;
      }
      STAMP(8);
    {
      const float Mp = __uint_as_float(ml.x), Lp = __uint_as_float(ml.z);
      float M = Mp; M = fmaxf(M, dppf<XOR1>(M)); M = fmaxf(M, dppf<XOR2>(M)); M = fmaxf(M, dppf<HMIR>(M)); M = fmaxf(M, dppf<ROR8>(M));
      const float wt = __builtin_amdgcn_exp2f(Mp - M);
      const float s0 = dppf<0x150>(wt), s1 = dppf<0x154>(wt), s2 = dppf<0x158>(wt), s3 = dppf<0x15C>(wt);
      float L = wt * Lp; L += dppf<XOR1>(L); L += dppf<XOR2>(L); L += dppf<HMIR>(L); L += dppf<ROR8>(L);
      float oa = fmaf(s1, __uint_as_float(v[1].x), s0 * __uint_as_float(v[0].x)) + fmaf(s3, __uint_as_float(v[3].x), s2 * __uint_as_float(v[2].x));
      float ob = fmaf(s1, __uint_as_float(v[1].z), s0 * __uint_as_float(v[0].z)) + fmaf(s3, __uint_as_float(v[3].z), s2 * __uint_as_float(v[2].z));
      const float rl = __builtin_amdgcn_rcpf(L);
      oa = x32sum(x16sum(oa)) * rl; ob = x32sum(x16sum(ob)) * rl;
      const unsigned o2 = cvtpk(oa, ob);
      if (lane < 16) *(LAS unsigned*)(L_O16 + r * 4) = o2;
      LGKM0();
      if (lane == 0) stv(L_ORDY, 1u);
      }
    } else if (c >= 2) {
      const float gate = __uint_as_float(ldv(L_GATE));
      const unsigned o_bs = opaque(gb_store_off(b, wg & 127, lane));
      if (c == 2) project<2, false>(lane, tag, gate, rs, o_bs); else project<4, false>(lane, tag, gate, rs, o_bs);
    } else if (c == 1) {
      const int fidx = kvh * 2 + (j >> 3);
      const unsigned q4 = r >> 2, p = lane & 3;
      float ys[7];
#pragma unroll
      for (int k = 0; k < 7; ++k) ys[k] = 0.f;
      if (fidx < 14) {
        const unsigned xa_addr = opaque(L_X + (lane & 1) * 896 + g * 8);
        const unsigned w1a = opaque(L_W1 + (4 * g + q4) * 32 + p * 8), w2a = opaque(L_W2 + (4 * g + q4) * (D * 2) + p * 8);
        u32x4 xf[14];
#pragma unroll
        for (int s = 0; s < 14; ++s) { const u32x2 a0 = ld64(xa_addr + s * 64), a1 = ld64(xa_addr + s * 64 + 32); xf[s] = u32x4{a0.x, a0.y, a1.x, a1.y}; }
        const float omg = __uint_as_float(ldv(L_OMG));
        WAIT_GE(L_LANDED, LD_W1);
        u32x2 bf[28];
#pragma unroll
        for (int s = 0; s < 14; ++s) { bf[2 * s] = trrd(w1a + s * 1024); bf[2 * s + 1] = trrd(w1a + s * 1024 + 512); }
        SCHED();
        f32x4 a0 = {0.f, 0.f, 0.f, 0.f}, a1 = {0.f, 0.f, 0.f, 0.f};
#pragma unroll
        for (int s = 0; s < 7; ++s) {
          a0 = mfma(xf[2 * s], u32x4{bf[4 * s].x, bf[4 * s].y, bf[4 * s + 1].x, bf[4 * s + 1].y}, a0);
          a1 = mfma(xf[2 * s + 1], u32x4{bf[4 * s + 2].x, bf[4 * s + 2].y, bf[4 * s + 3].x, bf[4 * s + 3].y}, a1);
        }
        float hv = rbf(b ? a0[1] + a1[1] : a0[0] + a1[0]);
        hv = hv / (1.f + __expf(-hv));
        *(LAS unsigned short*)(L_H16 + r * 2) = (unsigned short)f2bf(hv);
        LGKM0();
        const u32x2 hf = ld64(L_H16 + g * 8);
        WAIT_GE(L_LANDED, LD_W2);
        u32x2 w2f[28];
#pragma unroll
        for (int ct = 0; ct < 28; ++ct) w2f[ct] = trrd(w2a + ct * 32);
        SCHED();
#pragma unroll
        for (int k = 0; k < 7; ++k) {
          float y[4];
#pragma unroll
          for (int u = 0; u < 4; ++u)
            y[u] = __builtin_amdgcn_mfma_f32_16x16x16bf16_1k(__builtin_bit_cast(s16x4, hf), __builtin_bit_cast(s16x4, w2f[4 * k + u]), f32x4{0.f, 0.f, 0.f, 0.f}, 0, 0, 0)[0];
          ys[k] = omg * (sel4(g, y[0], y[1], y[2], y[3]));
        }
      }
      LGKM0();
#pragma unroll
      for (int k = 0; k < 7; ++k) *(LAS float*)(L_YB + (64 * k + lane) * 4) = ys[k];
      LGKM0();
      if (lane == 0) stv(L_YRDY, 1u);
      STAMP(24);
    }
  }
}

extern "C" void kernel_launch(void* const* d_in, const int* in_sizes, int n_in, void* d_out, int out_size, void* d_ws, size_t ws_size, hipStream_t stream) {
  (void)in_sizes; (void)n_in; (void)out_size; (void)ws_size;
  static bool attr = false;
  if (!attr) { (void)hipFuncSetAttribute((const void*)gated_mix_kernel, hipFuncAttributeMaxDynamicSharedMemorySize, L_BYTES); attr = true; }
  gated_mix_kernel<<<NWG, NTHR, L_BYTES, stream>>>((const unsigned short*)d_in[0], (const unsigned short*)d_in[1], (const unsigned short*)d_in[2],
                                                   (const unsigned short*)d_in[3], (const unsigned short*)d_in[4], (const unsigned short*)d_in[6],
                                                   (const unsigned short*)d_in[7], (const unsigned short*)d_in[8], (unsigned short*)d_out, (unsigned char*)d_ws);
}
```

```cpp
#include <hip/hip_runtime.h>
#include <stdint.h>
#include <type_traits>

#define LAS __attribute__((address_space(3)))
#define GAS __attribute__((address_space(1)))
typedef unsigned u32x4 __attribute__((ext_vector_type(4)));
typedef unsigned u32x2 __attribute__((ext_vector_type(2)));
typedef float f32x4 __attribute__((ext_vector_type(4)));
typedef short s16x4 __attribute__((ext_vector_type(4)));
typedef __bf16 bf16x8 __attribute__((ext_vector_type(8)));
typedef GAS unsigned long long gu64;
typedef GAS unsigned gu32;

constexpr int WLO = 16, WMID = 20, WHI = 28;
constexpr int THR = 100;
constexpr int CAL_A = 14, CAL_B = 30;
constexpr float CAL_EPS = 0.995f;
constexpr int T_Q = 450;

constexpr int D = 448, NQ = 4096, FF = 1792, SK = 32768;
constexpr int NWG = 256, NTHR = 384, NTILE = 64;
constexpr unsigned L_RING = 0;
constexpr unsigned L_R = 131072;
constexpr unsigned L_Q = 159744;
constexpr unsigned L_YB = 160768;
constexpr unsigned L_X = L_YB;
constexpr unsigned L_MISC = 162560;
constexpr unsigned L_LANDED = L_MISC, L_B1 = L_MISC + 4, L_ORDY = L_MISC + 8, L_YRDY = L_MISC + 12, L_CONS = L_MISC + 16, L_QRDY = L_MISC + 32,
                   L_TA = L_MISC + 40, L_TB = L_MISC + 44, L_FIN = L_MISC + 48, L_DEL = L_MISC + 52;
constexpr unsigned L_TAG = L_MISC + 64, L_OMG = L_MISC + 68, L_GATE = L_MISC + 72;
constexpr unsigned L_H16 = L_MISC + 96;
constexpr unsigned L_W1 = L_RING + 16384;
constexpr unsigned L_W2 = L_RING + 32768 + 16384;
constexpr unsigned LD_K63 = 16 + 32 * NTILE - 16, LD_V63 = 16 + 32 * NTILE, LD_W1 = LD_V63 + 16, LD_W2 = LD_V63 + 28, LD_ALL = 4096;
constexpr unsigned L_ML = L_MISC + 128;
constexpr unsigned L_O16 = L_MISC + 192;
constexpr unsigned L_BYTES = L_MISC + 256;
static_assert(L_BYTES <= 163840, "LDS");
constexpr size_t W_EPOCH = 0;
constexpr size_t W_GQ = 4096;
constexpr size_t W_GP = 40960;
constexpr unsigned W_ML = 65536;
constexpr unsigned W_GA = 131072;
constexpr unsigned W_GB = W_GA + (1u << 20);
constexpr unsigned W_END = W_GB + 2 * 112 * 128 * 32;
constexpr unsigned long long TMO = 25000000ull;

#pragma clang diagnostic ignored "-Wint-to-pointer-cast"
__device__ __forceinline__ unsigned long long rt() { return __builtin_amdgcn_s_memrealtime(); }
__device__ __forceinline__ float bf2f(unsigned b) { return __uint_as_float(b << 16); }
__device__ __forceinline__ unsigned f2bf(float f) { unsigned u = __float_as_uint(f); u += 0x7FFFu + ((u >> 16) & 1u); return u >> 16; }
__device__ __forceinline__ float rbf(float f) { return bf2f(f2bf(f)); }
__device__ __forceinline__ unsigned cvtpk(float lo, float hi) { unsigned r; asm("v_cvt_pk_bf16_f32 %0, %1, %2" : "=v"(r) : "v"(lo), "v"(hi)); return r; }
template <int CTRL> __device__ __forceinline__ float dppf(float x) { return __builtin_bit_cast(float, __builtin_amdgcn_mov_dpp(__builtin_bit_cast(int, x), CTRL, 0xf, 0xf, true)); }
template <int CTRL> __device__ __forceinline__ unsigned dppu(unsigned x) { return (unsigned)__builtin_amdgcn_mov_dpp((int)x, CTRL, 0xf, 0xf, true); }
constexpr int XOR1 = 0xB1, XOR2 = 0x4E, ROR4 = 0x124, ROR8 = 0x128, HMIR = 0x141;
__device__ __forceinline__ float x16max(float x) { auto s = __builtin_amdgcn_permlane16_swap(__float_as_uint(x), __float_as_uint(x), false, false); return fmaxf(__uint_as_float(s[0]), __uint_as_float(s[1])); }
__device__ __forceinline__ float x32max(float x) { auto s = __builtin_amdgcn_permlane32_swap(__float_as_uint(x), __float_as_uint(x), false, false); return fmaxf(__uint_as_float(s[0]), __uint_as_float(s[1])); }
__device__ __forceinline__ float x16sum(float x) { auto s = __builtin_amdgcn_permlane16_swap(__float_as_uint(x), __float_as_uint(x), false, false); return __uint_as_float(s[0]) + __uint_as_float(s[1]); }
__device__ __forceinline__ float x32sum(float x) { auto s = __builtin_amdgcn_permlane32_swap(__float_as_uint(x), __float_as_uint(x), false, false); return __uint_as_float(s[0]) + __uint_as_float(s[1]); }
__device__ __forceinline__ f32x4 mfma(u32x4 a, u32x4 b, f32x4 c) { return __builtin_amdgcn_mfma_f32_16x16x32_bf16(__builtin_bit_cast(bf16x8, a), __builtin_bit_cast(bf16x8, b), c, 0, 0, 0); }
__device__ __forceinline__ u32x2 trrd(unsigned addr) { return __builtin_bit_cast(u32x2, __builtin_amdgcn_ds_read_tr16_b64_v4i16((LAS s16x4*)addr)); }
__device__ __forceinline__ u32x4 ld128(unsigned addr) { return *(const LAS u32x4*)addr; }
__device__ __forceinline__ u32x2 ld64(unsigned addr) { return *(const LAS u32x2*)addr; }
__device__ __forceinline__ float ldf(unsigned addr) { return *(const LAS float*)addr; }
__device__ __forceinline__ unsigned ldv(unsigned addr) { return *(volatile LAS unsigned*)addr; }
__device__ __forceinline__ unsigned ldu(unsigned addr) { return __builtin_amdgcn_readfirstlane(*(volatile LAS unsigned*)addr); }
__device__ __forceinline__ void stv(unsigned addr, unsigned v) { *(volatile LAS unsigned*)addr = v; }
__device__ __forceinline__ void st1(unsigned addr, unsigned v) {
  unsigned a, t;
  asm volatile("s_mov_b64 exec, 1\n\tv_mov_b32 %0, %2\n\tv_mov_b32 %1, %3\n\tds_write_b32 %0, %1\n\ts_mov_b64 exec, -1" : "=&v"(a), "=&v"(t) : "s"(addr), "s"(v) : "memory");
}
__device__ __forceinline__ float sel4(unsigned g, float a0, float a1, float a2, float a3) { const bool b0 = g & 1, b1 = g & 2; const float lo = b0 ? a1 : a0, hi = b0 ? a3 : a2; return b1 ? hi : lo; }
__device__ __forceinline__ unsigned opaque(unsigned x) { asm volatile("" : "+v"(x)); return x; }
__device__ __forceinline__ u32x4 mk_rsrc(const void* p, unsigned bytes) {
  const unsigned long long a = (unsigned long long)p;
  u32x4 r; r.x = (unsigned)a; r.y = (unsigned)(a >> 32) & 0xffffu; r.z = bytes; r.w = 0x00020000u; return r;
}
__device__ __forceinline__ void st_gran(gu64* g, unsigned tag, unsigned v) { __hip_atomic_store(g, ((unsigned long long)tag << 32) | v, __ATOMIC_RELAXED, __HIP_MEMORY_SCOPE_AGENT); }
__device__ __forceinline__ unsigned long long ld_gran(gu64* g) { return __hip_atomic_load(g, __ATOMIC_RELAXED, __HIP_MEMORY_SCOPE_AGENT); }
__device__ __forceinline__ __amdgpu_buffer_rsrc_t ws_rsrc(unsigned char* ws) { return __builtin_amdgcn_make_buffer_rsrc(ws, 0, W_END, 0x00020000); }
__device__ __forceinline__ void st_gran2(__amdgpu_buffer_rsrc_t rs, unsigned off, unsigned tag, float v0, float v1) {
  __builtin_amdgcn_raw_buffer_store_b128(u32x4{__float_as_uint(v0), tag, __float_as_uint(v1), tag}, rs, off, 0, 16);
}
__device__ __forceinline__ u32x4 ld_gran2(__amdgpu_buffer_rsrc_t rs, unsigned off) { return __builtin_amdgcn_raw_buffer_load_b128(rs, off, 0, 16); }
#define LGKM0() asm volatile("s_waitcnt lgkmcnt(0)" ::: "memory")
#define VM0() asm volatile("s_waitcnt vmcnt(0)" ::: "memory")
#define BAR() asm volatile("s_barrier" ::: "memory")
#define WAIT_GE(a, v) do { while (ldu(a) < (unsigned)(v)) __builtin_amdgcn_s_sleep(1); asm volatile("" ::: "memory"); } while (0)
#define SPIN_GE(a, v) do { while (ldu(a) < (unsigned)(v)) { } asm volatile("" ::: "memory"); } while (0)
#define SCHED() __builtin_amdgcn_sched_barrier(0)
template <int N> __device__ __forceinline__ void vmwait() { asm volatile("s_waitcnt vmcnt(%0)" :: "i"(N) : "memory"); }

__device__ __forceinline__ void dma4(unsigned m0v, unsigned v0, unsigned v1, unsigned v2, unsigned v3, u32x4 rs, unsigned soff) {
  asm volatile("s_mov_b32 m0, %0\n\ts_nop 0\n\t"
               "buffer_load_dwordx4 %1, %5, %6 offen nt lds\n\t"
               "buffer_load_dwordx4 %2, %5, %6 offen offset:1024 nt lds\n\t"
               "buffer_load_dwordx4 %3, %5, %6 offen offset:2048 nt lds\n\t"
               "buffer_load_dwordx4 %4, %5, %6 offen offset:3072 nt lds"
               :: "s"(m0v), "v"(v0), "v"(v1), "v"(v2), "v"(v3), "s"(rs), "s"(soff) : "memory");
}
__device__ __forceinline__ void dma1(unsigned m0v, unsigned v0, u32x4 rs, unsigned soff) {
  asm volatile("s_mov_b32 m0, %0\n\ts_nop 0\n\tbuffer_load_dwordx4 %1, %2, %3 offen nt lds" :: "s"(m0v), "v"(v0), "s"(rs), "s"(soff) : "memory");
}

__device__ __forceinline__ u32x4 qk_softmax(const u32x4 (&kf)[16], const u32x4 (&qf)[8], float& m, float& l, f32x4 (&acc)[16]) {
  constexpr float C2 = 0.0625f * 1.4426950408889634f;
  f32x4 s0 = {0.f, 0.f, 0.f, 0.f}, s1 = {0.f, 0.f, 0.f, 0.f};
#pragma unroll
  for (int s = 0; s < 8; ++s) { s0 = mfma(kf[s], qf[s], s0); s1 = mfma(kf[8 + s], qf[s], s1); }
  float tm = fmaxf(fmaxf(fmaxf(s0[0], s0[1]), fmaxf(s0[2], s0[3])), fmaxf(fmaxf(s1[0], s1[1]), fmaxf(s1[2], s1[3])));
  tm = x32max(x16max(tm)) * C2;
  if (__any(tm > m)) {
    const float mn = fmaxf(m, tm);
    const float alpha = __builtin_amdgcn_exp2f(m - mn);
    m = mn; l *= alpha;
    const float al0 = dppf<0x00>(alpha), al1 = dppf<0x55>(alpha);
#pragma unroll
    for (int n = 0; n < 16; ++n) { acc[n][0] *= al0; acc[n][1] *= al1; }
  }
  float p[8];
#pragma unroll
  for (int i = 0; i < 4; ++i) { p[i] = __builtin_amdgcn_exp2f(fmaf(s0[i], C2, -m)); p[4 + i] = __builtin_amdgcn_exp2f(fmaf(s1[i], C2, -m)); }
  l += ((p[0] + p[1]) + (p[2] + p[3])) + ((p[4] + p[5]) + (p[6] + p[7]));
  u32x4 pf; pf.x = cvtpk(p[0], p[1]); pf.y = cvtpk(p[2], p[3]); pf.z = cvtpk(p[4], p[5]); pf.w = cvtpk(p[6], p[7]);
  return pf;
}

template <int K0, bool LAST>
__device__ __forceinline__ void project(int lane, unsigned tag, float gate, __amdgpu_buffer_rsrc_t rs, unsigned o_bs) {
  constexpr int NK = LAST ? 3 : 2;
  const unsigned g = lane >> 4, r = lane & 15, q4 = r >> 2, p = lane & 3;
  const unsigned wa = opaque(L_R + (8 * g + q4) * (D * 2) + p * 8), o16r = opaque(L_O16 + g * 16);
  WAIT_GE(L_LANDED, LD_ALL);
  u32x4 wf[4 * NK];
  float ybv[NK];
#pragma unroll
  for (int kk = 0; kk < NK; ++kk)
#pragma unroll
    for (int u = 0; u < 4; ++u) {
      const int ct = 4 * (kk < 2 ? K0 + kk : 6) + u;
      const u32x2 b0 = trrd(wa + ct * 32), b1 = trrd(wa + ct * 32 + 4 * D * 2);
      wf[4 * kk + u] = u32x4{b0.x, b0.y, b1.x, b1.y};
    }
  WAIT_GE(L_YRDY, 1u);
#pragma unroll
  for (int kk = 0; kk < NK; ++kk) ybv[kk] = ldf(L_YB + (64 * (kk < 2 ? K0 + kk : 6) + lane) * 4);
  LGKM0();
  SPIN_GE(L_ORDY, 1u);
  const u32x4 af = ld128(o16r);
  float ys[NK];
#pragma unroll
  for (int kk = 0; kk < NK; ++kk) {
    float y[4];
#pragma unroll
    for (int u = 0; u < 4; ++u) y[u] = mfma(af, wf[4 * kk + u], f32x4{0.f, 0.f, 0.f, 0.f})[0];
    ys[kk] = fmaf(gate, sel4(g, y[0], y[1], y[2], y[3]), ybv[kk]);
  }
  const float an = dppf<XOR1>(ys[0]), bn = dppf<XOR1>(ys[1]);
  st_gran2(rs, o_bs + K0 * 16 * 4096, tag, (lane & 1) ? bn : ys[0], (lane & 1) ? ys[1] : an);
  if (LAST) {
    const float cn = dppf<XOR1>(ys[NK - 1]);
    if (!(lane & 1)) st_gran2(rs, o_bs + 6 * 16 * 4096, tag, ys[NK - 1], cn);
  }
}
__device__ __forceinline__ unsigned gb_store_off(int b, int fp, int lane) {
  return W_GB + (unsigned)(b * 112 * 128 + fp) * 32u + (unsigned)((((lane & 1) * 64 + (lane & ~1)) >> 2) * 4096 + (lane & 2) * 8);
}

__global__ void __launch_bounds__(NTHR, 2)
gated_mix_kernel(const unsigned short* __restrict__ xa, const unsigned short* __restrict__ kc, const unsigned short* __restrict__ vc,
                 const unsigned short* __restrict__ wq, const unsigned short* __restrict__ wo, const unsigned short* __restrict__ w1,
                 const unsigned short* __restrict__ w2, const unsigned short* __restrict__ gw, unsigned short* __restrict__ out,
                 unsigned char* __restrict__ ws) {
  const int tid = threadIdx.x, lane = tid & 63;
  const int wave = __builtin_amdgcn_readfirstlane(tid >> 6);
  const int wg = blockIdx.x;
  const int grp = wg >> 4, j = wg & 15, b = grp >> 3, kvh = grp & 7;
  const unsigned cs = (wg & 7) * 32 + (wg >> 3);

  if (wave == 0) {

    __builtin_amdgcn_s_setprio(3);
    {
      const u32x4 rq = mk_rsrc(wq, D * NQ * 2);
      const unsigned vo = (lane >> 1) * (NQ * 2) + (lane & 1) * 16;
#pragma unroll
      for (int i = 0; i < 14; ++i) dma1(L_R + i * 1024, vo, rq, cs * 32 + i * 32 * (NQ * 2));
    }
    const u32x4 rk = mk_rsrc(kc, 256u << 20), rv = mk_rsrc(vc, 256u << 20);
    unsigned vk[8], vv[4];
    const unsigned hi = lane >> 5, pc = lane & 31;
#pragma unroll
    for (int i = 0; i < 8; ++i) vk[i] = hi * 512 + ((pc ^ (2 * i + hi)) << 4);
#pragma unroll
    for (int i = 0; i < 4; ++i) vv[i] = hi * 512 + ((pc ^ ((2 * i + hi) << 1)) << 4);
    const unsigned base = (unsigned)(grp * SK + j * 2048) * 512u;
    int pub = 0;
    unsigned del = 0, fin = 0;
    int mode = 1, mode_next = 1;
    bool free_next = true;
    auto pick = [&](int u) {
      if (u <= 2 * CAL_B + 1) {
        if (u == 2 * CAL_A + 1) st1(L_TA, (unsigned)rt() | 1u);
        if (u == 2 * CAL_B + 1) st1(L_TB, (unsigned)rt() | 1u);
        return;
      }
      if (!del) { del = ldu(L_DEL); fin = ldu(L_FIN); if (!del) return; }
      const int lag = (int)((unsigned)rt() - fin) + (int)(((2 * NTILE + 1 - u) * del) >> 4);
      mode_next = lag < -THR ? 0 : lag > THR ? 2 : 1;
    };
    auto half = [&](auto wc, auto vc_, int t, unsigned slot, unsigned so) {
      constexpr int W = decltype(wc)::value; constexpr bool ISV = decltype(vc_)::value;
#pragma unroll
      for (int gi = 0; gi < 4; ++gi) {
        vmwait<W - 4>();
        if constexpr (ISV) dma4(slot + 16384 + gi * 4096, vv[0], vv[1], vv[2], vv[3], rv, so + gi * 4096);
        else if (gi & 1) dma4(slot + gi * 4096, vk[4], vk[5], vk[6], vk[7], rk, so + gi * 4096);
        else dma4(slot + gi * 4096, vk[0], vk[1], vk[2], vk[3], rk, so + gi * 4096);
        if (((4 * gi - (W - 4)) & 15) == 0) {
          const int cand = 16 + 32 * t + (ISV ? 16 : 0) + 4 * gi - (W - 4);
          if (cand > pub) { pub = cand; st1(L_LANDED, (unsigned)cand); }
        }
        if (gi == 0) {
          if constexpr (!ISV) { if (t == 0) BAR(); }
          pick(2 * t + (ISV ? 2 : 1));
          if constexpr (ISV) free_next = t < 3 || ldu(L_CONS + ((t + 1) & 3) * 4) >= (unsigned)((t + 1) >> 2);
        }
      }
    };
    using W0 = std::integral_constant<int, WLO>; using W1 = std::integral_constant<int, WMID>; using W2 = std::integral_constant<int, WHI>;
    using KK = std::integral_constant<bool, false>; using VV = std::integral_constant<bool, true>;
    for (int t = 0; t < NTILE; ++t) {
      const unsigned slot = L_RING + (t & 3) * 32768u;
      const unsigned so = base + t * 16384u;
      if (!free_next) {
        const unsigned need = t >> 2;
        if (ldu(L_CONS + (t & 3) * 4) < need) {
          VM0();
          pub = 16 + 32 * t; st1(L_LANDED, (unsigned)pub);
          WAIT_GE(L_CONS + (t & 3) * 4, need);
        }
      }
      if (mode == 1) half(W1{}, KK{}, t, slot, so); else if (mode == 0) half(W0{}, KK{}, t, slot, so); else half(W2{}, KK{}, t, slot, so);
      mode = mode_next;
      if (mode == 1) half(W1{}, VV{}, t, slot, so); else if (mode == 0) half(W0{}, VV{}, t, slot, so); else half(W2{}, VV{}, t, slot, so);
      mode = mode_next;
    }
    {
      const int fidx = kvh * 2 + (j >> 3), fs = (j & 7) * 14 + fidx;
      const u32x4 r1 = mk_rsrc(w1, D * FF * 2), r2 = mk_rsrc(w2, FF * D * 2), ro = mk_rsrc(wo, NQ * D * 2);
      const unsigned vo1 = (lane >> 1) * (FF * 2) + (lane & 1) * 16, vol = lane * 16;
      const unsigned so1 = fs * 32, so2 = fs * 16 * (D * 2), soo = (kvh * 512 + j * 32) * (D * 2);
      auto pubx = [&](int cand) { if (cand > pub) { pub = cand; st1(L_LANDED, (unsigned)cand); } };
      vmwait<16>(); pubx(LD_K63);
      if (fidx < 14) {
        WAIT_GE(L_CONS, NTILE / 4); WAIT_GE(L_CONS + 4, NTILE / 4);
#pragma unroll
        for (int e = 0; e < 14; ++e) dma1(L_W1 + e * 1024, vo1, r1, so1 + e * 32 * (FF * 2));
#pragma unroll
        for (int e = 0; e < 3; ++e) dma4(L_W2 + e * 4096, vol, vol, vol, vol, r2, so2 + e * 4096);
        dma1(L_W2 + 12 * 1024, vol, r2, so2 + 12 * 1024); dma1(L_W2 + 13 * 1024, vol, r2, so2 + 13 * 1024);
        vmwait<28>(); pubx(LD_V63);
#pragma unroll
        for (int e = 0; e < 7; ++e) dma4(L_R + e * 4096, vol, vol, vol, vol, ro, soo + e * 4096);
        vmwait<42>(); pubx(LD_W1);
        vmwait<28>(); pubx(LD_W2);
      } else {
#pragma unroll
        for (int e = 0; e < 7; ++e) dma4(L_R + e * 4096, vol, vol, vol, vol, ro, soo + e * 4096);
        vmwait<28>(); pubx(LD_V63);
      }
      VM0();
      st1(L_LANDED, LD_ALL);
    }

    return;
  }

  if (wave == 1) {

    const unsigned t0 = (unsigned)rt();
    const unsigned g = lane >> 4, r = lane & 15, q4 = r >> 2, p = lane & 3;
    const unsigned epoch = __hip_atomic_load((gu32*)(ws + W_EPOCH), __ATOMIC_RELAXED, __HIP_MEMORY_SCOPE_AGENT);
    const u32x4 x0 = *((const GAS u32x4*)xa + lane);
    const u32x4 x1 = *((const GAS u32x4*)xa + 64 + (lane < 48 ? lane : 47));
    const u32x4 gwv = *((const GAS u32x4*)gw + (lane < 56 ? lane : 55));
    u32x4 pw[9];
    {
      const __amdgpu_buffer_rsrc_t rs0 = ws_rsrc(ws);
      const unsigned o_val = W_GA + (unsigned)((grp * 16 + j) * 16 + g) * 256u + r * 16u;
      const unsigned o_ml = W_ML + (unsigned)((grp * 16 + j) * 16 + r) * 16u;
      const unsigned o_bl = W_GB + (unsigned)((b * 112 + ((wg & 127) < 112 ? (wg & 127) : 0)) * 128) * 32u + lane * 16u;
#pragma unroll
      for (int i = 0; i < 4; ++i) { pw[i] = ld_gran2(rs0, o_val + i * 1024); pw[5 + i] = ld_gran2(rs0, o_bl + i * 1024); }
      pw[4] = ld_gran2(rs0, o_ml);
    }
    if (lane < 16) stv(L_MISC + lane * 4, 0u);
    LGKM0();
    BAR();
    *(LAS u32x4*)(L_X + lane * 16) = x0;
    if (lane < 48) *(LAS u32x4*)(L_X + 1024 + lane * 16) = x1;
    const unsigned tag = __builtin_amdgcn_readfirstlane(epoch) + 1u;
    if (lane == 0) stv(L_TAG, tag);
    LGKM0();
    const unsigned xa_addr = opaque(L_X + (lane & 1) * 896 + g * 8);
    const unsigned wt_addr = opaque(L_R + (4 * g + q4) * 32 + p * 8);
    u32x4 xf[14];
#pragma unroll
    for (int s = 0; s < 14; ++s) { const u32x2 a0 = ld64(xa_addr + s * 64), a1 = ld64(xa_addr + s * 64 + 32); xf[s] = u32x4{a0.x, a0.y, a1.x, a1.y}; }
    float gate;
    {
      const u32x4 xv = ld128(L_X + b * 896 + (lane < 56 ? lane : 55) * 16);
      float s = 0.f;
#pragma unroll
      for (int k = 0; k < 4; ++k) { s += rbf(bf2f(xv[k] & 0xffffu) * bf2f(gwv[k] & 0xffffu)); s += rbf(__uint_as_float(xv[k] & 0xffff0000u) * __uint_as_float(gwv[k] & 0xffff0000u)); }
      if (lane >= 56) s = 0.f;
      s += dppf<XOR1>(s); s += dppf<XOR2>(s); s += dppf<HMIR>(s); s += dppf<ROR8>(s); s = x32sum(x16sum(s));
      s = rbf(s);
      gate = rbf(1.f / (1.f + __expf(-s)));
      if (lane == 0) { stv(L_OMG, __float_as_uint(rbf(1.f - gate))); stv(L_GATE, __float_as_uint(gate)); }
    }
    auto slice_gemv = [&]() -> f32x4 {
      u32x2 bf[28];
#pragma unroll
      for (int s = 0; s < 14; ++s) { bf[2 * s] = trrd(wt_addr + s * 1024); bf[2 * s + 1] = trrd(wt_addr + s * 1024 + 512); }
      SCHED();
      f32x4 a0 = {0.f, 0.f, 0.f, 0.f}, a1 = {0.f, 0.f, 0.f, 0.f};
#pragma unroll
      for (int s = 0; s < 7; ++s) {
        a0 = mfma(xf[2 * s], u32x4{bf[4 * s].x, bf[4 * s].y, bf[4 * s + 1].x, bf[4 * s + 1].y}, a0);
        a1 = mfma(xf[2 * s + 1], u32x4{bf[4 * s + 2].x, bf[4 * s + 2].y, bf[4 * s + 3].x, bf[4 * s + 3].y}, a1);
      }
      return a0 + a1;
    };
    WAIT_GE(L_LANDED, 16);

    {
      const f32x4 a = slice_gemv();
      const unsigned bits = f2bf((g & 1) ? a[1] : a[0]);
      const unsigned nb = dppu<XOR1>(bits);
      if (g < 2 && !(lane & 1)) st_gran((gu64*)(ws + W_GQ) + g * 2048 + cs * 8 + (r >> 1), tag, bits | (nb << 16));
    }

#pragma unroll
    for (int i = 0; i < 9; ++i) asm volatile("" :: "v"(pw[i]));
    while ((int)((unsigned)rt() - t0) < T_Q) __builtin_amdgcn_s_sleep(4);
    {
      gu64* gq = (gu64*)(ws + W_GQ) + b * 2048 + kvh * 256 + lane;
      unsigned v[4];
      for (;;) {
        bool ok = true;
#pragma unroll
        for (int i = 0; i < 4; ++i) { const unsigned long long x = ld_gran(gq + i * 64); v[i] = (unsigned)x; ok &= (unsigned)(x >> 32) == tag; }
        if (__all(ok)) break;
        if ((unsigned)rt() - t0 > (unsigned)TMO) break;
        __builtin_amdgcn_s_sleep(8);
      }
#pragma unroll
      for (int i = 0; i < 4; ++i) *(LAS unsigned*)(L_Q + (i * 64 + lane) * 4) = v[i];
      LGKM0();
      if (lane == 0) stv(L_QRDY, 1u);
    }

    {
      while (ldu(L_TB) == 0) __builtin_amdgcn_s_sleep(16);
      const unsigned ta = ldu(L_TA), tb = ldu(L_TB);
      gu64* gp = (gu64*)(ws + W_GP) + grp * 32;
      if (lane < 2) st_gran(gp + j * 2 + lane, tag, lane ? tb : ta);
      while ((int)((unsigned)rt() - tb) < 250) __builtin_amdgcn_s_sleep(16);
      for (int tries = 0; tries < 12; ++tries) {
        const unsigned long long x = ld_gran(gp + (lane & 31));
        if (__all((unsigned)(x >> 32) == tag)) {
          float d = (float)(int)((unsigned)x - tb);
          d += dppf<XOR2>(d); d += dppf<ROR4>(d); d += dppf<ROR8>(d); d = x16sum(d);
          const float ma = __builtin_bit_cast(float, __builtin_amdgcn_readlane(__builtin_bit_cast(int, d), 0)) * (1.f / 16.f);
          const float mb = __builtin_bit_cast(float, __builtin_amdgcn_readlane(__builtin_bit_cast(int, d), 1)) * (1.f / 16.f);
          const float dh = (mb - ma) * (CAL_EPS / (2 * (CAL_B - CAL_A)));
          if (lane == 0) stv(L_FIN, tb + (unsigned)(int)(mb + dh * (2 * (NTILE - CAL_B))));
          LGKM0();
          if (lane == 0) stv(L_DEL, (unsigned)(dh * 16.f + 0.5f));
          break;
        }
        for (int s = 0; s < 8; ++s) __builtin_amdgcn_s_sleep(16);
      }
    }
    const __amdgpu_buffer_rsrc_t rs = ws_rsrc(ws);
    const int fp = wg & 127;
    unsigned o_bs = opaque(W_GB + (unsigned)(b * 112 * 128 + fp) * 32u + (unsigned)((((lane & 1) * 64 + (lane & ~1)) >> 2) * 4096 + (lane & 2) * 8));
    unsigned o_bl = opaque(W_GB + (unsigned)((b * 112 + (fp < 112 ? fp : 0)) * 128) * 32u + lane * 16u);
    project<0, true>(lane, tag, gate, rs, o_bs);

    const unsigned long long t1 = rt();
    if (fp < 112) {
      u32x4 u[4];
      unsigned spins = 0;
      for (;;) {
        asm volatile("" ::: "memory");
#pragma unroll
        for (int i = 0; i < 4; ++i) u[i] = ld_gran2(rs, o_bl + i * 1024);
        const unsigned bad = ((u[0].y ^ tag) | (u[0].w ^ tag) | (u[1].y ^ tag)) | ((u[1].w ^ tag) | (u[2].y ^ tag) | (u[2].w ^ tag)) | ((u[3].y ^ tag) | (u[3].w ^ tag));
        if (__all(bad == 0)) break;
        if ((++spins & 63u) == 0 && rt() - t1 > TMO) break;
      }
      float sa = (__uint_as_float(u[0].x) + __uint_as_float(u[1].x)) + (__uint_as_float(u[2].x) + __uint_as_float(u[3].x));
      float sb = (__uint_as_float(u[0].z) + __uint_as_float(u[1].z)) + (__uint_as_float(u[2].z) + __uint_as_float(u[3].z));
      sa += dppf<XOR2>(sa); sb += dppf<XOR2>(sb); sa += dppf<ROR4>(sa); sb += dppf<ROR4>(sb); sa += dppf<ROR8>(sa); sb += dppf<ROR8>(sb);
      sa = x32sum(x16sum(sa)); sb = x32sum(x16sum(sb));
      if (lane < 2) *(GAS unsigned*)(out + b * D + fp * 4 + lane * 2) = cvtpk(sa, sb);
      if (wg == 0 && lane == 0) __hip_atomic_store((gu32*)(ws + W_EPOCH), tag, __ATOMIC_RELAXED, __HIP_MEMORY_SCOPE_AGENT);
    }

    return;
  }

  {
    const int c = wave - 2;
    const unsigned g = lane >> 4, r = lane & 15;
    BAR();
    const unsigned slot = L_RING + c * 32768u;
    unsigned ka[4], va[8];
#pragma unroll
    for (int s = 0; s < 4; ++s) ka[s] = opaque(slot + r * 512 + (((4 * s + g) ^ r) << 4));
    {
      const unsigned q4 = r >> 2, p = lane & 3, row = 4 * g + q4;
      const unsigned X = (row & 7) << 1;
#pragma unroll
      for (int n = 0; n < 8; ++n) va[n] = opaque(slot + 16384 + row * 512 + (((2 * n + (p >> 1)) ^ X) << 4) + (p & 1) * 8);
    }
    u32x4 qf[8];
    f32x4 acc[16];
    float m = -INFINITY, l = 0.f;
    {
      u32x4 kf[16]; u32x2 vf[32];
      WAIT_GE(L_LANDED, 16 + 32 * c + 16);
#pragma unroll
      for (int s = 0; s < 8; ++s) { kf[s] = ld128(ka[s & 3] + (s >> 2) * 256); kf[8 + s] = ld128(ka[s & 3] + (s >> 2) * 256 + 8192); }
      WAIT_GE(L_LANDED, 16 + 32 * c + 32);
#pragma unroll
      for (int n = 0; n < 16; ++n) { vf[2 * n] = trrd(va[n & 7] + (n >> 3) * 256); vf[2 * n + 1] = trrd(va[n & 7] + (n >> 3) * 256 + 8192); }
      LGKM0();
      if (lane == 0) stv(L_CONS + c * 4, 1u);
      WAIT_GE(L_QRDY, 1u);
#pragma unroll
      for (int s = 0; s < 8; ++s) qf[s] = ld128(L_Q + (lane & 1) * 512 + s * 64 + g * 16);
      SCHED();
#pragma unroll
      for (int n = 0; n < 16; ++n) acc[n] = f32x4{0.f, 0.f, 0.f, 0.f};
      const u32x4 pf = qk_softmax(kf, qf, m, l, acc);
#pragma unroll
      for (int n = 0; n < 16; ++n) { u32x4 a; a.x = vf[2 * n].x; a.y = vf[2 * n].y; a.z = vf[2 * n + 1].x; a.w = vf[2 * n + 1].y; acc[n] = mfma(pf, a, acc[n]); }
    }
    const unsigned tag = ldv(L_TAG);
    const __amdgpu_buffer_rsrc_t rs = ws_rsrc(ws);
    float lt = 0.f;
    float pre[4][2], wt3[2] = {0.f, 0.f}, Mf[2] = {0.f, 0.f}, Lf[2] = {0.f, 0.f};
#pragma unroll
    for (int i = 0; i < 4; ++i) { pre[i][0] = 0.f; pre[i][1] = 0.f; }
    const unsigned pa = opaque((64 * g + r) * 4);
    for (int it = 1; it < NTILE / 4; ++it) {
      const unsigned t = c + 4 * it;
      WAIT_GE(L_LANDED, 16 + 32 * t + 16);
      u32x4 pf;
      {
        u32x4 kf[16];
#pragma unroll
        for (int s = 0; s < 8; ++s) { kf[s] = ld128(ka[s & 3] + (s >> 2) * 256); kf[8 + s] = ld128(ka[s & 3] + (s >> 2) * 256 + 8192); }
        SCHED();
        pf = qk_softmax(kf, qf, m, l, acc);
      }
      lt = x32sum(x16sum(l));
      if (it == NTILE / 4 - 1) {
        const float mo[2] = {dppf<0x00>(m), dppf<0x55>(m)}, lo[2] = {dppf<0x00>(lt), dppf<0x55>(lt)};
        if (c > 0) {
          WAIT_GE(L_B1, c);
          const f32x4 ml = *(const LAS f32x4*)(L_ML + (c - 1) * 16);
          float a[4][2];
#pragma unroll
          for (int i = 0; i < 4; ++i) { a[i][0] = ldf(slot - 32768 + pa + i * 64); a[i][1] = ldf(slot - 32768 + 1024 + pa + i * 64); }
#pragma unroll
          for (int h = 0; h < 2; ++h) {
            const float M = fmaxf(ml[2 * h], mo[h]);
            const float wt = __builtin_amdgcn_exp2f(ml[2 * h] - M);
            wt3[h] = __builtin_amdgcn_exp2f(mo[h] - M);
            Mf[h] = M; Lf[h] = fmaf(wt, ml[2 * h + 1], wt3[h] * lo[h]);
#pragma unroll
            for (int i = 0; i < 4; ++i) pre[i][h] = wt * a[i][h];
          }
        } else {
#pragma unroll
          for (int h = 0; h < 2; ++h) { wt3[h] = 1.f; Mf[h] = mo[h]; Lf[h] = lo[h]; }
        }
        asm volatile("" : "+v"(pre[0][0]), "+v"(pre[0][1]), "+v"(pre[1][0]), "+v"(pre[1][1]), "+v"(pre[2][0]), "+v"(pre[2][1]), "+v"(pre[3][0]), "+v"(pre[3][1]),
                          "+v"(wt3[0]), "+v"(wt3[1]), "+v"(Mf[0]), "+v"(Mf[1]), "+v"(Lf[0]), "+v"(Lf[1]));
        SPIN_GE(L_LANDED, 16 + 32 * t + 32);
      } else {
        WAIT_GE(L_LANDED, 16 + 32 * t + 32);
      }
      u32x2 vf[32];
#pragma unroll
      for (int n = 0; n < 16; ++n) { vf[2 * n] = trrd(va[n & 7] + (n >> 3) * 256); vf[2 * n + 1] = trrd(va[n & 7] + (n >> 3) * 256 + 8192); }
      SCHED();
#pragma unroll
      for (int n = 0; n < 16; ++n) { u32x4 a; a.x = vf[2 * n].x; a.y = vf[2 * n].y; a.z = vf[2 * n + 1].x; a.w = vf[2 * n + 1].y; acc[n] = mfma(pf, a, acc[n]); }
      LGKM0();
      if (lane == 0) stv(L_CONS + c * 4, it + 1);
    }
    {
      float val[4][2];
#pragma unroll
      for (int i = 0; i < 4; ++i)
#pragma unroll
        for (int h = 0; h < 2; ++h) val[i][h] = fmaf(wt3[h], sel4(g, acc[i][h], acc[4 + i][h], acc[8 + i][h], acc[12 + i][h]), pre[i][h]);
      if (c < 3) {
#pragma unroll
        for (int i = 0; i < 4; ++i) { *(LAS float*)(slot + pa + i * 64) = val[i][0]; *(LAS float*)(slot + 1024 + pa + i * 64) = val[i][1]; }
        if (lane == 0) *(LAS f32x4*)(L_ML + c * 16) = f32x4{Mf[0], Lf[0], Mf[1], Lf[1]};
        LGKM0();
        if (lane == 0) stv(L_B1, c + 1);
      } else {
        const unsigned h = lane & 1;
        const unsigned o_st = W_GA + (unsigned)((grp * 16 + h * 8 + 2 * g) * 16 + j) * 256u + (r >> 1) * 16u;
#pragma unroll
        for (int i = 0; i < 4; ++i) {
          const float n0 = dppf<XOR1>(val[i][0]), n1 = dppf<XOR1>(val[i][1]);
          st_gran2(rs, o_st + (i >> 1) * 4096 + (i & 1) * 128, tag, h ? n1 : val[i][0], h ? val[i][1] : n0);
        }
        if (lane < 16) st_gran2(rs, W_ML + (unsigned)((grp * 16 + lane) * 16 + j) * 16u, tag, lane < 8 ? Mf[0] : Mf[1], lane < 8 ? Lf[0] : Lf[1]);

      }
    }
    if (c == 0) {
      const unsigned o_val = opaque(W_GA + (unsigned)((grp * 16 + j) * 16 + g) * 256u + r * 16u);
      const unsigned o_ml = opaque(W_ML + (unsigned)((grp * 16 + j) * 16 + ((r + g) & 15)) * 16u);
      WAIT_GE(L_LANDED, LD_V63);
      const unsigned long long t1 = rt();
      unsigned spins = 0;
      u32x4 v[4], ml;
      for (;;) {
        asm volatile("" ::: "memory");
#pragma unroll
        for (int i = 0; i < 4; ++i) v[i] = ld_gran2(rs, o_val + i * 1024);
        ml = ld_gran2(rs, o_ml);
        const unsigned bad = ((v[0].y ^ tag) | (v[0].w ^ tag) | (v[1].y ^ tag)) | ((v[1].w ^ tag) | (v[2].y ^ tag) | (v[2].w ^ tag)) |
                             ((v[3].y ^ tag) | (v[3].w ^ tag) | (ml.y ^ tag)) | (ml.w ^ tag);
        if (__all(bad == 0)) break;
        if ((++spins & 63u) == 0 && rt() - t1 > TMO) break;
      }

    {
      const float Mp = __uint_as_float(ml.x), Lp = __uint_as_float(ml.z);
      float M = Mp; M = fmaxf(M, dppf<XOR1>(M)); M = fmaxf(M, dppf<XOR2>(M)); M = fmaxf(M, dppf<HMIR>(M)); M = fmaxf(M, dppf<ROR8>(M));
      const float wt = __builtin_amdgcn_exp2f(Mp - M);
      const float s0 = dppf<0x150>(wt), s1 = dppf<0x154>(wt), s2 = dppf<0x158>(wt), s3 = dppf<0x15C>(wt);
      float L = wt * Lp; L += dppf<XOR1>(L); L += dppf<XOR2>(L); L += dppf<HMIR>(L); L += dppf<ROR8>(L);
      float oa = fmaf(s1, __uint_as_float(v[1].x), s0 * __uint_as_float(v[0].x)) + fmaf(s3, __uint_as_float(v[3].x), s2 * __uint_as_float(v[2].x));
      float ob = fmaf(s1, __uint_as_float(v[1].z), s0 * __uint_as_float(v[0].z)) + fmaf(s3, __uint_as_float(v[3].z), s2 * __uint_as_float(v[2].z));
      const float rl = __builtin_amdgcn_rcpf(L);
      oa = x32sum(x16sum(oa)) * rl; ob = x32sum(x16sum(ob)) * rl;
      const unsigned o2 = cvtpk(oa, ob);
      if (lane < 16) *(LAS unsigned*)(L_O16 + r * 4) = o2;
      LGKM0();
      if (lane == 0) stv(L_ORDY, 1u);
      }
    } else if (c >= 2) {
      const float gate = __uint_as_float(ldv(L_GATE));
      const unsigned o_bs = opaque(gb_store_off(b, wg & 127, lane));
      if (c == 2) project<2, false>(lane, tag, gate, rs, o_bs); else project<4, false>(lane, tag, gate, rs, o_bs);
    } else if (c == 1) {
      const int fidx = kvh * 2 + (j >> 3);
      const unsigned q4 = r >> 2, p = lane & 3;
      float ys[7];
#pragma unroll
      for (int k = 0; k < 7; ++k) ys[k] = 0.f;
      if (fidx < 14) {
        const unsigned xa_addr = opaque(L_X + (lane & 1) * 896 + g * 8);
        const unsigned w1a = opaque(L_W1 + (4 * g + q4) * 32 + p * 8), w2a = opaque(L_W2 + (4 * g + q4) * (D * 2) + p * 8);
        u32x4 xf[14];
#pragma unroll
        for (int s = 0; s < 14; ++s) { const u32x2 a0 = ld64(xa_addr + s * 64), a1 = ld64(xa_addr + s * 64 + 32); xf[s] = u32x4{a0.x, a0.y, a1.x, a1.y}; }
        const float omg = __uint_as_float(ldv(L_OMG));
        WAIT_GE(L_LANDED, LD_W1);
        u32x2 bf[28];
#pragma unroll
        for (int s = 0; s < 14; ++s) { bf[2 * s] = trrd(w1a + s * 1024); bf[2 * s + 1] = trrd(w1a + s * 1024 + 512); }
        SCHED();
        f32x4 a0 = {0.f, 0.f, 0.f, 0.f}, a1 = {0.f, 0.f, 0.f, 0.f};
#pragma unroll
        for (int s = 0; s < 7; ++s) {
          a0 = mfma(xf[2 * s], u32x4{bf[4 * s].x, bf[4 * s].y, bf[4 * s + 1].x, bf[4 * s + 1].y}, a0);
          a1 = mfma(xf[2 * s + 1], u32x4{bf[4 * s + 2].x, bf[4 * s + 2].y, bf[4 * s + 3].x, bf[4 * s + 3].y}, a1);
        }
        float hv = rbf(b ? a0[1] + a1[1] : a0[0] + a1[0]);
        hv = hv / (1.f + __expf(-hv));
        *(LAS unsigned short*)(L_H16 + r * 2) = (unsigned short)f2bf(hv);
        LGKM0();
        const u32x2 hf = ld64(L_H16 + g * 8);
        WAIT_GE(L_LANDED, LD_W2);
        u32x2 w2f[28];
#pragma unroll
        for (int ct = 0; ct < 28; ++ct) w2f[ct] = trrd(w2a + ct * 32);
        SCHED();
#pragma unroll
        for (int k = 0; k < 7; ++k) {
          float y[4];
#pragma unroll
          for (int u = 0; u < 4; ++u)
            y[u] = __builtin_amdgcn_mfma_f32_16x16x16bf16_1k(__builtin_bit_cast(s16x4, hf), __builtin_bit_cast(s16x4, w2f[4 * k + u]), f32x4{0.f, 0.f, 0.f, 0.f}, 0, 0, 0)[0];
          ys[k] = omg * (sel4(g, y[0], y[1], y[2], y[3]));
        }
      }
      LGKM0();
#pragma unroll
      for (int k = 0; k < 7; ++k) *(LAS float*)(L_YB + (64 * k + lane) * 4) = ys[k];
      LGKM0();
      if (lane == 0) stv(L_YRDY, 1u);

    }
  }
}

extern "C" void kernel_launch(void* const* d_in, const int* in_sizes, int n_in, void* d_out, int out_size, void* d_ws, size_t ws_size, hipStream_t stream) {
  (void)in_sizes; (void)n_in; (void)out_size; (void)ws_size;
  (void)hipFuncSetAttribute((const void*)gated_mix_kernel, hipFuncAttributeMaxDynamicSharedMemorySize, L_BYTES);
  gated_mix_kernel<<<NWG, NTHR, L_BYTES, stream>>>((const unsigned short*)d_in[0], (const unsigned short*)d_in[1], (const unsigned short*)d_in[2],
                                                   (const unsigned short*)d_in[3], (const unsigned short*)d_in[4], (const unsigned short*)d_in[6],
                                                   (const unsigned short*)d_in[7], (const unsigned short*)d_in[8], (unsigned short*)d_out, (unsigned char*)d_ws);
}
```

```cpp
#include <hip/hip_runtime.h>
#include <stdint.h>
#include <type_traits>

#define LAS __attribute__((address_space(3)))
#define GAS __attribute__((address_space(1)))
typedef unsigned u32x4 __attribute__((ext_vector_type(4)));
typedef unsigned u32x2 __attribute__((ext_vector_type(2)));
typedef float f32x4 __attribute__((ext_vector_type(4)));
typedef short s16x4 __attribute__((ext_vector_type(4)));
typedef __bf16 bf16x8 __attribute__((ext_vector_type(8)));
typedef GAS unsigned long long gu64;
typedef GAS unsigned gu32;

constexpr int WLO = 16, WMID = 24, WHI = 32;
constexpr int THR = 60;
constexpr int DEL0 = 872, OFF0 = 120;
constexpr int CAL_A = 14, CAL_B = 30;
constexpr int UA2 = 66, UB2 = 88;
constexpr float CAL_EPS = 0.995f, CAL_EPS2 = 1.0f;
constexpr int T_Q = 450;

constexpr int D = 448, NQ = 4096, FF = 1792, SK = 32768;
constexpr int NWG = 256, NTHR = 384, NTILE = 64;
constexpr unsigned L_RING = 0;
constexpr unsigned L_R = 131072;
constexpr unsigned L_S = L_R;
constexpr unsigned L_PART = L_R + 8192;
constexpr unsigned L_Q = 145408;
constexpr unsigned L_YB = 146432;
constexpr unsigned L_X = L_YB;
constexpr unsigned L_MISC = 148224;
constexpr unsigned L_LANDED = L_MISC, L_B1 = L_MISC + 4, L_ORDY = L_MISC + 8, L_YRDY = L_MISC + 12, L_CONS = L_MISC + 16, L_QRDY = L_MISC + 32,
                   L_TA = L_MISC + 40, L_TB = L_MISC + 44, L_FIN = L_MISC + 48, L_DEL = L_MISC + 52, L_MCNT = L_MISC + 56, L_TA2 = L_MISC + 36, L_TB2 = L_MISC + 60;
constexpr unsigned L_TAG = L_MISC + 64, L_OMG = L_MISC + 68, L_GATE = L_MISC + 72;
constexpr unsigned L_H16 = L_MISC + 96;
constexpr unsigned L_W1 = L_RING;
constexpr unsigned L_W2 = L_RING + 16384;
constexpr unsigned L_WO = L_RING + 32768;
constexpr unsigned LD_K63 = 16 + 32 * NTILE - 16, LD_V63 = 16 + 32 * NTILE, LD_W1 = LD_V63 + 16, LD_W2 = LD_V63 + 28, LD_ALL = 4096;
constexpr unsigned L_MW = L_MISC + 128;
constexpr unsigned L_LR = L_MISC + 160;
constexpr unsigned L_O16 = L_MISC + 192;
constexpr unsigned L_BYTES = L_MISC + 256;
static_assert(L_BYTES <= 163840 && L_BYTES > 81920, "LDS: one workgroup per CU");
constexpr size_t W_EPOCH = 0;
constexpr size_t W_GQ = 4096;
constexpr size_t W_GP = 40960;
constexpr unsigned W_ML = 65536;
constexpr unsigned W_GA = 131072;
constexpr int CELL = 4, NCELL = 448 / CELL;
constexpr unsigned W_GB = W_GA + (1u << 20);
constexpr unsigned W_END = W_GB + 2 * 448 * 128 * 8;
constexpr unsigned long long TMO = 25000000ull;

#pragma clang diagnostic ignored "-Wint-to-pointer-cast"
__device__ __forceinline__ unsigned long long rt() { return __builtin_amdgcn_s_memrealtime(); }
__device__ __forceinline__ float bf2f(unsigned b) { return __uint_as_float(b << 16); }
__device__ __forceinline__ unsigned f2bf(float f) { unsigned u = __float_as_uint(f); u += 0x7FFFu + ((u >> 16) & 1u); return u >> 16; }
__device__ __forceinline__ float rbf(float f) { return bf2f(f2bf(f)); }
typedef __bf16 bf16x2 __attribute__((ext_vector_type(2)));
__device__ __forceinline__ unsigned cvtpk(float lo, float hi) { const bf16x2 v = {(__bf16)lo, (__bf16)hi}; return __builtin_bit_cast(unsigned, v); }
template <int CTRL> __device__ __forceinline__ float dppf(float x) { return __builtin_bit_cast(float, __builtin_amdgcn_mov_dpp(__builtin_bit_cast(int, x), CTRL, 0xf, 0xf, true)); }
template <int CTRL> __device__ __forceinline__ unsigned dppu(unsigned x) { return (unsigned)__builtin_amdgcn_mov_dpp((int)x, CTRL, 0xf, 0xf, true); }
constexpr int XOR1 = 0xB1, XOR2 = 0x4E, ROR4 = 0x124, ROR8 = 0x128, HMIR = 0x141;
__device__ __forceinline__ float x16max(float x) { auto s = __builtin_amdgcn_permlane16_swap(__float_as_uint(x), __float_as_uint(x), false, false); return fmaxf(__uint_as_float(s[0]), __uint_as_float(s[1])); }
__device__ __forceinline__ float x32max(float x) { auto s = __builtin_amdgcn_permlane32_swap(__float_as_uint(x), __float_as_uint(x), false, false); return fmaxf(__uint_as_float(s[0]), __uint_as_float(s[1])); }
__device__ __forceinline__ float x16sum(float x) { auto s = __builtin_amdgcn_permlane16_swap(__float_as_uint(x), __float_as_uint(x), false, false); return __uint_as_float(s[0]) + __uint_as_float(s[1]); }
__device__ __forceinline__ float x32sum(float x) { auto s = __builtin_amdgcn_permlane32_swap(__float_as_uint(x), __float_as_uint(x), false, false); return __uint_as_float(s[0]) + __uint_as_float(s[1]); }
__device__ __forceinline__ f32x4 mfma(u32x4 a, u32x4 b, f32x4 c) { return __builtin_amdgcn_mfma_f32_16x16x32_bf16(__builtin_bit_cast(bf16x8, a), __builtin_bit_cast(bf16x8, b), c, 0, 0, 0); }
__device__ __forceinline__ u32x2 trrd(unsigned addr) { return __builtin_bit_cast(u32x2, __builtin_amdgcn_ds_read_tr16_b64_v4i16((LAS s16x4*)addr)); }
__device__ __forceinline__ u32x4 ld128(unsigned addr) { return *(const LAS u32x4*)addr; }
__device__ __forceinline__ u32x2 ld64(unsigned addr) { return *(const LAS u32x2*)addr; }
__device__ __forceinline__ float ldf(unsigned addr) { return *(const LAS float*)addr; }
__device__ __forceinline__ unsigned ldv(unsigned addr) { return *(volatile LAS unsigned*)addr; }
__device__ __forceinline__ unsigned ldu(unsigned addr) { return __builtin_amdgcn_readfirstlane(*(volatile LAS unsigned*)addr); }
__device__ __forceinline__ void stv(unsigned addr, unsigned v) { *(volatile LAS unsigned*)addr = v; }
__device__ __forceinline__ void st1(unsigned addr, unsigned v) {
  unsigned a, t;
  asm volatile("s_mov_b64 exec, 1\n\tv_mov_b32 %0, %2\n\tv_mov_b32 %1, %3\n\tds_write_b32 %0, %1\n\ts_mov_b64 exec, -1" : "=&v"(a), "=&v"(t) : "s"(addr), "s"(v) : "memory");
}
__device__ __forceinline__ float sel4(unsigned g, float a0, float a1, float a2, float a3) { const bool b0 = g & 1, b1 = g & 2; const float lo = b0 ? a1 : a0, hi = b0 ? a3 : a2; return b1 ? hi : lo; }
__device__ __forceinline__ unsigned opaque(unsigned x) { asm volatile("" : "+v"(x)); return x; }
__device__ __forceinline__ u32x4 mk_rsrc(const void* p, unsigned bytes) {
  const unsigned long long a = (unsigned long long)p;
  u32x4 r; r.x = (unsigned)a; r.y = (unsigned)(a >> 32) & 0xffffu; r.z = bytes; r.w = 0x00020000u; return r;
}
__device__ __forceinline__ void st_gran(gu64* g, unsigned tag, unsigned v) { __hip_atomic_store(g, ((unsigned long long)tag << 32) | v, __ATOMIC_RELAXED, __HIP_MEMORY_SCOPE_AGENT); }
__device__ __forceinline__ unsigned long long ld_gran(gu64* g) { return __hip_atomic_load(g, __ATOMIC_RELAXED, __HIP_MEMORY_SCOPE_AGENT); }
__device__ __forceinline__ __amdgpu_buffer_rsrc_t ws_rsrc(unsigned char* ws) { return __builtin_amdgcn_make_buffer_rsrc(ws, 0, W_END, 0x00020000); }
__device__ __forceinline__ void st_gran2(__amdgpu_buffer_rsrc_t rs, unsigned off, unsigned tag, float v0, float v1) {
  __builtin_amdgcn_raw_buffer_store_b128(u32x4{__float_as_uint(v0), tag, __float_as_uint(v1), tag}, rs, off, 0, 16);
}
__device__ __forceinline__ u32x4 ld_gran2(__amdgpu_buffer_rsrc_t rs, unsigned off) { return __builtin_amdgcn_raw_buffer_load_b128(rs, off, 0, 16); }
#define LGKM0() asm volatile("s_waitcnt lgkmcnt(0)" ::: "memory")
#define VM0() asm volatile("s_waitcnt vmcnt(0)" ::: "memory")
#define BAR() asm volatile("s_barrier" ::: "memory")
#define WAIT_GE(a, v) do { while (ldu(a) < (unsigned)(v)) __builtin_amdgcn_s_sleep(1); asm volatile("" ::: "memory"); } while (0)
#define SPIN_GE(a, v) do { while (ldu(a) < (unsigned)(v)) { } asm volatile("" ::: "memory"); } while (0)
#define SCHED() __builtin_amdgcn_sched_barrier(0)
template <int N> __device__ __forceinline__ void vmwait() { asm volatile("s_waitcnt vmcnt(%0)" :: "i"(N) : "memory"); }

__device__ __forceinline__ void dma4(unsigned m0v, unsigned v0, unsigned v1, unsigned v2, unsigned v3, u32x4 rs, unsigned soff) {
  asm volatile("s_mov_b32 m0, %0\n\ts_nop 0\n\t"
               "buffer_load_dwordx4 %1, %5, %6 offen nt lds\n\t"
               "buffer_load_dwordx4 %2, %5, %6 offen offset:1024 nt lds\n\t"
               "buffer_load_dwordx4 %3, %5, %6 offen offset:2048 nt lds\n\t"
               "buffer_load_dwordx4 %4, %5, %6 offen offset:3072 nt lds"
               :: "s"(m0v), "v"(v0), "v"(v1), "v"(v2), "v"(v3), "s"(rs), "s"(soff) : "memory");
}
__device__ __forceinline__ void dma1(unsigned m0v, unsigned v0, u32x4 rs, unsigned soff) {
  asm volatile("s_mov_b32 m0, %0\n\ts_nop 0\n\tbuffer_load_dwordx4 %1, %2, %3 offen nt lds" :: "s"(m0v), "v"(v0), "s"(rs), "s"(soff) : "memory");
}

__device__ __forceinline__ u32x4 qk_scores(const u32x4 (&kf)[16], const u32x4 (&qf)[8], float& mx) {
  f32x4 s0 = {0.f, 0.f, 0.f, 0.f}, s1 = {0.f, 0.f, 0.f, 0.f};
#pragma unroll
  for (int s = 0; s < 8; ++s) { s0 = mfma(kf[s], qf[s], s0); s1 = mfma(kf[8 + s], qf[s], s1); }
  mx = fmaxf(mx, fmaxf(fmaxf(fmaxf(s0[0], s0[1]), fmaxf(s0[2], s0[3])), fmaxf(fmaxf(s1[0], s1[1]), fmaxf(s1[2], s1[3]))));
  u32x4 sp; sp.x = cvtpk(s0[0], s0[1]); sp.y = cvtpk(s0[2], s0[3]); sp.z = cvtpk(s1[0], s1[1]); sp.w = cvtpk(s1[2], s1[3]);
  return sp;
}

template <int K0, bool LAST>
__device__ __forceinline__ void project(int lane, unsigned tag, float gate, __amdgpu_buffer_rsrc_t rs, unsigned o_bs) {
  constexpr int NK = LAST ? 3 : 2;
  const unsigned g = lane >> 4, r = lane & 15, q4 = r >> 2, p = lane & 3;
  const unsigned wa = opaque(L_WO + (8 * g + q4) * (D * 2) + p * 8), o16r = opaque(L_O16 + g * 16);
  WAIT_GE(L_LANDED, LD_ALL);
  u32x4 wf[4 * NK];
  float ybv[NK];
#pragma unroll
  for (int kk = 0; kk < NK; ++kk)
#pragma unroll
    for (int u = 0; u < 4; ++u) {
      const int ct = 4 * (kk < 2 ? K0 + kk : 6) + u;
      const u32x2 b0 = trrd(wa + ct * 32), b1 = trrd(wa + ct * 32 + 4 * D * 2);
      wf[4 * kk + u] = u32x4{b0.x, b0.y, b1.x, b1.y};
    }
  WAIT_GE(L_YRDY, 1u);
#pragma unroll
  for (int kk = 0; kk < NK; ++kk) ybv[kk] = ldf(L_YB + (64 * (kk < 2 ? K0 + kk : 6) + lane) * 4);
  LGKM0();
  SPIN_GE(L_ORDY, 1u);
  const u32x4 af = ld128(o16r);
  float ys[NK];
#pragma unroll
  for (int kk = 0; kk < NK; ++kk) {
    float y[4];
#pragma unroll
    for (int u = 0; u < 4; ++u) y[u] = mfma(af, wf[4 * kk + u], f32x4{0.f, 0.f, 0.f, 0.f})[0];
    ys[kk] = fmaf(gate, sel4(g, y[0], y[1], y[2], y[3]), ybv[kk]);
  }
  const float an = dppf<XOR1>(ys[0]), bn = dppf<XOR1>(ys[1]);
  st_gran2(rs, o_bs + K0 * (64 / CELL) * (128 * CELL * 8), tag, (lane & 1) ? bn : ys[0], (lane & 1) ? ys[1] : an);
  if (LAST) {
    const float cn = dppf<XOR1>(ys[NK - 1]);
    if (!(lane & 1)) st_gran2(rs, o_bs + 6 * (64 / CELL) * (128 * CELL * 8), tag, ys[NK - 1], cn);
  }
}
__device__ __forceinline__ unsigned gb_store_off(int b, int fp, int lane) {
  const unsigned n = (lane & 1) * 64 + (lane & ~1);
  return W_GB + (unsigned)(b * NCELL * 128 + fp) * (CELL * 8u) + (n / CELL) * (128u * CELL * 8u) + (n % CELL) * 8u;
}

__global__ void __launch_bounds__(NTHR, 2)
gated_mix_kernel(const unsigned short* __restrict__ xa, const unsigned short* __restrict__ kc, const unsigned short* __restrict__ vc,
                 const unsigned short* __restrict__ wq, const unsigned short* __restrict__ wo, const unsigned short* __restrict__ w1,
                 const unsigned short* __restrict__ w2, const unsigned short* __restrict__ gw, unsigned short* __restrict__ out,
                 unsigned char* __restrict__ ws) {
  const int tid = threadIdx.x, lane = tid & 63;
  const int wave = __builtin_amdgcn_readfirstlane(tid >> 6);
  const int wg = blockIdx.x;
  const int grp = wg >> 4, j = wg & 15, b = grp >> 3, kvh = grp & 7;
  const unsigned cs = (wg & 7) * 32 + (wg >> 3);

  if (wave == 0) {
    __builtin_amdgcn_s_setprio(3);
    {
      const u32x4 rq = mk_rsrc(wq, D * NQ * 2);
      const unsigned vo = (lane >> 1) * (NQ * 2) + (lane & 1) * 16;
#pragma unroll
      for (int i = 0; i < 14; ++i) dma1(L_R + i * 1024, vo, rq, cs * 32 + i * 32 * (NQ * 2));
    }
    const u32x4 rk = mk_rsrc(kc, 256u << 20), rv = mk_rsrc(vc, 256u << 20);
    unsigned vk[8], vv[4];
    const unsigned hi = lane >> 5, pc = lane & 31;
#pragma unroll
    for (int i = 0; i < 8; ++i) vk[i] = hi * 512 + ((pc ^ (2 * i + hi)) << 4);
#pragma unroll
    for (int i = 0; i < 4; ++i) vv[i] = hi * 512 + ((pc ^ ((2 * i + hi) << 1)) << 4);
    const unsigned base = (unsigned)(grp * SK + j * 2048) * 512u;
    int pub = 0;
    unsigned del = 0, fin = 0;
    const unsigned tstart = (unsigned)rt();
    int mode = 1, mode_next = 1;
    bool free_next = true;
    auto pick = [&](int u) {
      if (u == 2 * CAL_A + 1) st1(L_TA, (unsigned)rt() | 1u);
      if (u == 2 * CAL_B + 1) st1(L_TB, (unsigned)rt() | 1u);
      if (u == UA2) st1(L_TA2, (unsigned)rt() | 1u);
      if (u == UB2) st1(L_TB2, (unsigned)rt() | 1u);
      if (u > 2 * CAL_B + 1) { const u32x2 sc = *(volatile LAS u32x2*)L_FIN; fin = __builtin_amdgcn_readfirstlane(sc.x); del = __builtin_amdgcn_readfirstlane(sc.y); }
      const int lag = del ? (int)((unsigned)rt() - fin) + (int)(((2 * NTILE + 1 - u) * del) >> 4) : (int)((unsigned)rt() - tstart) - OFF0 - ((u * DEL0) >> 4);
      mode_next = lag < -THR ? 0 : lag > THR ? 2 : 1;
    };
    auto half = [&](auto wc, auto vc_, int p, unsigned slot, unsigned so) {
      constexpr int W = decltype(wc)::value; constexpr bool ISV = decltype(vc_)::value;
#pragma unroll
      for (int gi = 0; gi < 4; ++gi) {
        vmwait<W - 4>();
        if constexpr (ISV) dma4(slot + gi * 4096, vv[0], vv[1], vv[2], vv[3], rv, so + gi * 4096);
        else if (gi & 1) dma4(slot + gi * 4096, vk[4], vk[5], vk[6], vk[7], rk, so + gi * 4096);
        else dma4(slot + gi * 4096, vk[0], vk[1], vk[2], vk[3], rk, so + gi * 4096);
        if (((4 * gi - (W - 4)) & 15) == 0) {
          const int cand = 16 + 16 * p + 4 * gi - (W - 4);
          if (cand > pub) { pub = cand; st1(L_LANDED, (unsigned)cand); }
        }
        if (gi == 0) {
          if constexpr (!ISV) { if (p == 0) BAR(); }
          pick(p + 1);
          free_next = p < 7 || ldu(L_CONS + ((p + 1) & 3) * 4) >= (unsigned)(((p + 1) >> 2) - 1);
        }
      }
    };
    using W0 = std::integral_constant<int, WLO>; using W1 = std::integral_constant<int, WMID>; using W2 = std::integral_constant<int, WHI>;
    using KK = std::integral_constant<bool, false>; using VV = std::integral_constant<bool, true>;
    for (int p = 0; p < 2 * NTILE; ++p) {
      const unsigned slot = L_RING + (p & 7) * 16384u;
      const unsigned so = base + (p & (NTILE - 1)) * 16384u;
      if (!free_next) {
        const unsigned need = (p >> 2) - 1;
        if (ldu(L_CONS + (p & 3) * 4) < need) {
          VM0();
          pub = 16 + 16 * p; st1(L_LANDED, (unsigned)pub);
          WAIT_GE(L_CONS + (p & 3) * 4, need);
        }
      }
      if (p < NTILE) { if (mode == 1) half(W1{}, KK{}, p, slot, so); else if (mode == 0) half(W0{}, KK{}, p, slot, so); else half(W2{}, KK{}, p, slot, so); }
      else { if (mode == 1) half(W1{}, VV{}, p, slot, so); else if (mode == 0) half(W0{}, VV{}, p, slot, so); else half(W2{}, VV{}, p, slot, so); }
      mode = mode_next;
    }
    {
      const int fidx = kvh * 2 + (j >> 3), fs = (j & 7) * 14 + fidx;
      const u32x4 r1 = mk_rsrc(w1, D * FF * 2), r2 = mk_rsrc(w2, FF * D * 2), ro = mk_rsrc(wo, NQ * D * 2);
      const unsigned vo1 = (lane >> 1) * (FF * 2) + (lane & 1) * 16, vol = lane * 16;
      const unsigned so1 = fs * 32, so2 = fs * 16 * (D * 2), soo = (kvh * 512 + j * 32) * (D * 2);
      auto pubx = [&](int cand) { if (cand > pub) { pub = cand; st1(L_LANDED, (unsigned)cand); } };
      vmwait<16>(); pubx(LD_K63);
      WAIT_GE(L_CONS, 31u); WAIT_GE(L_CONS + 4, 31u); WAIT_GE(L_CONS + 8, 31u); WAIT_GE(L_CONS + 12, 31u);
      if (fidx < 14) {
#pragma unroll
        for (int e = 0; e < 14; ++e) dma1(L_W1 + e * 1024, vo1, r1, so1 + e * 32 * (FF * 2));
#pragma unroll
        for (int e = 0; e < 3; ++e) dma4(L_W2 + e * 4096, vol, vol, vol, vol, r2, so2 + e * 4096);
        dma1(L_W2 + 12 * 1024, vol, r2, so2 + 12 * 1024); dma1(L_W2 + 13 * 1024, vol, r2, so2 + 13 * 1024);
        vmwait<28>(); pubx(LD_V63);
#pragma unroll
        for (int e = 0; e < 7; ++e) dma4(L_WO + e * 4096, vol, vol, vol, vol, ro, soo + e * 4096);
        vmwait<42>(); pubx(LD_W1);
        vmwait<28>(); pubx(LD_W2);
      } else {
#pragma unroll
        for (int e = 0; e < 7; ++e) dma4(L_WO + e * 4096, vol, vol, vol, vol, ro, soo + e * 4096);
        vmwait<28>(); pubx(LD_V63);
      }
      VM0();
      st1(L_LANDED, LD_ALL);
    }
    return;
  }

  if (wave == 1) {
    const unsigned t0 = (unsigned)rt();
    const unsigned g = lane >> 4, r = lane & 15, q4 = r >> 2, p = lane & 3;
    const unsigned epoch = __hip_atomic_load((gu32*)(ws + W_EPOCH), __ATOMIC_RELAXED, __HIP_MEMORY_SCOPE_AGENT);
    const u32x4 x0 = *((const GAS u32x4*)xa + lane);
    const u32x4 x1 = *((const GAS u32x4*)xa + 64 + (lane < 48 ? lane : 47));
    const u32x4 gwv = *((const GAS u32x4*)gw + (lane < 56 ? lane : 55));
    u32x4 pw[9];
    {
      const __amdgpu_buffer_rsrc_t rs0 = ws_rsrc(ws);
      const unsigned o_val = W_GA + (unsigned)((grp * 16 + j) * 16 + g) * 256u + r * 16u;
      const unsigned o_ml = W_ML + (unsigned)((grp * 16 + j) * 16 + r) * 16u;
      const unsigned o_bl = W_GB + (unsigned)((b * NCELL + ((wg & 127) < NCELL ? (wg & 127) : 0)) * 128) * (CELL * 8u) + lane * 16u;
#pragma unroll
      for (int i = 0; i < 4; ++i) { pw[i] = ld_gran2(rs0, o_val + i * 1024); pw[5 + i] = ld_gran2(rs0, o_bl + i * 1024); }
      pw[4] = ld_gran2(rs0, o_ml);
    }
    if (lane < 16) stv(L_MISC + lane * 4, 0u);
    LGKM0();
    BAR();
    *(LAS u32x4*)(L_X + lane * 16) = x0;
    if (lane < 48) *(LAS u32x4*)(L_X + 1024 + lane * 16) = x1;
    const unsigned tag = __builtin_amdgcn_readfirstlane(epoch) + 1u;
    if (lane == 0) stv(L_TAG, tag);
    LGKM0();
    const unsigned xa_addr = opaque(L_X + (lane & 1) * 896 + g * 8);
    const unsigned wt_addr = opaque(L_R + (4 * g + q4) * 32 + p * 8);
    u32x4 xf[14];
#pragma unroll
    for (int s = 0; s < 14; ++s) { const u32x2 a0 = ld64(xa_addr + s * 64), a1 = ld64(xa_addr + s * 64 + 32); xf[s] = u32x4{a0.x, a0.y, a1.x, a1.y}; }
    float gate;
    {
      const u32x4 xv = ld128(L_X + b * 896 + (lane < 56 ? lane : 55) * 16);
      float s = 0.f;
#pragma unroll
      for (int k = 0; k < 4; ++k) { s += rbf(bf2f(xv[k] & 0xffffu) * bf2f(gwv[k] & 0xffffu)); s += rbf(__uint_as_float(xv[k] & 0xffff0000u) * __uint_as_float(gwv[k] & 0xffff0000u)); }
      if (lane >= 56) s = 0.f;
      s += dppf<XOR1>(s); s += dppf<XOR2>(s); s += dppf<HMIR>(s); s += dppf<ROR8>(s); s = x32sum(x16sum(s));
      s = rbf(s);
      gate = rbf(1.f / (1.f + __expf(-s)));
      if (lane == 0) { stv(L_OMG, __float_as_uint(rbf(1.f - gate))); stv(L_GATE, __float_as_uint(gate)); }
    }
    auto slice_gemv = [&]() -> f32x4 {
      u32x2 bf[28];
#pragma unroll
      for (int s = 0; s < 14; ++s) { bf[2 * s] = trrd(wt_addr + s * 1024); bf[2 * s + 1] = trrd(wt_addr + s * 1024 + 512); }
      SCHED();
      f32x4 a0 = {0.f, 0.f, 0.f, 0.f}, a1 = {0.f, 0.f, 0.f, 0.f};
#pragma unroll
      for (int s = 0; s < 7; ++s) {
        a0 = mfma(xf[2 * s], u32x4{bf[4 * s].x, bf[4 * s].y, bf[4 * s + 1].x, bf[4 * s + 1].y}, a0);
        a1 = mfma(xf[2 * s + 1], u32x4{bf[4 * s + 2].x, bf[4 * s + 2].y, bf[4 * s + 3].x, bf[4 * s + 3].y}, a1);
      }
      return a0 + a1;
    };
    WAIT_GE(L_LANDED, 16);
    {
      const f32x4 a = slice_gemv();
      const unsigned bits = f2bf((g & 1) ? a[1] : a[0]);
      const unsigned nb = dppu<XOR1>(bits);
      if (g < 2 && !(lane & 1)) st_gran((gu64*)(ws + W_GQ) + g * 2048 + cs * 8 + (r >> 1), tag, bits | (nb << 16));
    }
#pragma unroll
    for (int i = 0; i < 9; ++i) asm volatile("" :: "v"(pw[i]));
    while ((int)((unsigned)rt() - t0) < T_Q) __builtin_amdgcn_s_sleep(4);
    {
      gu64* gq = (gu64*)(ws + W_GQ) + b * 2048 + kvh * 256 + lane;
      unsigned v[4];
      for (;;) {
        bool ok = true;
#pragma unroll
        for (int i = 0; i < 4; ++i) { const unsigned long long x = ld_gran(gq + i * 64); v[i] = (unsigned)x; ok &= (unsigned)(x >> 32) == tag; }
        if (__all(ok)) break;
        if ((unsigned)rt() - t0 > (unsigned)TMO) break;
        __builtin_amdgcn_s_sleep(8);
      }
#pragma unroll
      for (int i = 0; i < 4; ++i) *(LAS unsigned*)(L_Q + (i * 64 + lane) * 4) = v[i];
      LGKM0();
      if (lane == 0) stv(L_QRDY, 1u);
    }
#pragma nounroll
    for (int round = 0; round < 2; ++round) {
      const unsigned la = round ? L_TA2 : L_TA, lb = round ? L_TB2 : L_TB;
      const int ua = round ? UA2 : 2 * CAL_A + 1, ub = round ? UB2 : 2 * CAL_B + 1;
      while (ldu(lb) == 0) __builtin_amdgcn_s_sleep(16);
      const unsigned ta = ldu(la), tb = ldu(lb);
      gu64* gp = (gu64*)(ws + W_GP) + round * 512 + grp * 32;
      if (lane < 2) st_gran(gp + j * 2 + lane, tag, lane ? tb : ta);
      while ((int)((unsigned)rt() - tb) < 250) __builtin_amdgcn_s_sleep(16);
      for (int tries = 0; tries < 12; ++tries) {
        const unsigned long long x = ld_gran(gp + (lane & 31));
        if (__all((unsigned)(x >> 32) == tag)) {
          float d = (float)(int)((unsigned)x - tb);
          d += dppf<XOR2>(d); d += dppf<ROR4>(d); d += dppf<ROR8>(d); d = x16sum(d);
          const float ma = __builtin_bit_cast(float, __builtin_amdgcn_readlane(__builtin_bit_cast(int, d), 0)) * (1.f / 16.f);
          const float mb = __builtin_bit_cast(float, __builtin_amdgcn_readlane(__builtin_bit_cast(int, d), 1)) * (1.f / 16.f);
          const float dh = (mb - ma) * (round ? CAL_EPS2 : CAL_EPS) / (float)(ub - ua);
          if (lane == 0) *(volatile LAS u32x2*)L_FIN = u32x2{tb + (unsigned)(int)(mb + dh * (float)(2 * NTILE + 1 - ub)), (unsigned)(dh * 16.f + 0.5f)};
          break;
        }
        for (int s = 0; s < 8; ++s) __builtin_amdgcn_s_sleep(16);
      }
    }
    const __amdgpu_buffer_rsrc_t rs = ws_rsrc(ws);
    const int fp = wg & 127;
    const unsigned o_bs = opaque(gb_store_off(b, fp, lane));
    const unsigned o_bl = opaque(W_GB + (unsigned)((b * NCELL + (fp < NCELL ? fp : 0)) * 128) * (CELL * 8u) + lane * 16u);
    project<0, true>(lane, tag, gate, rs, o_bs);
    const unsigned long long t1 = rt();
    if (fp < NCELL) {
      u32x4 u[4];
      unsigned spins = 0;
      for (;;) {
        asm volatile("" ::: "memory");
#pragma unroll
        for (int i = 0; i < 4; ++i) u[i] = ld_gran2(rs, o_bl + i * 1024);
        const unsigned bad = ((u[0].y ^ tag) | (u[0].w ^ tag) | (u[1].y ^ tag)) | ((u[1].w ^ tag) | (u[2].y ^ tag) | (u[2].w ^ tag)) | ((u[3].y ^ tag) | (u[3].w ^ tag));
        if (__all(bad == 0)) break;
        if ((++spins & 63u) == 0 && rt() - t1 > TMO) break;
      }
      float sa = (__uint_as_float(u[0].x) + __uint_as_float(u[1].x)) + (__uint_as_float(u[2].x) + __uint_as_float(u[3].x));
      float sb = (__uint_as_float(u[0].z) + __uint_as_float(u[1].z)) + (__uint_as_float(u[2].z) + __uint_as_float(u[3].z));
      sa += dppf<XOR2>(sa); sb += dppf<XOR2>(sb); sa += dppf<ROR4>(sa); sb += dppf<ROR4>(sb); sa += dppf<ROR8>(sa); sb += dppf<ROR8>(sb);
      sa = x32sum(x16sum(sa)); sb = x32sum(x16sum(sb));
      if (lane < 2) *(GAS unsigned*)(out + b * D + fp * 4 + lane * 2) = cvtpk(sa, sb);
      if (wg == 0 && lane == 0) __hip_atomic_store((gu32*)(ws + W_EPOCH), tag, __ATOMIC_RELAXED, __HIP_MEMORY_SCOPE_AGENT);
    }
    return;
  }

  {
    const int c = wave - 2;
    const unsigned g = lane >> 4, r = lane & 15;
    BAR();
    const unsigned slot = L_RING + c * 16384u;
    unsigned ka[4], va[8];
#pragma unroll
    for (int s = 0; s < 4; ++s) ka[s] = opaque(slot + r * 512 + (((4 * s + g) ^ r) << 4));
    {
      const unsigned q4 = r >> 2, p = lane & 3, row = 4 * g + q4;
      const unsigned X = (row & 7) << 1;
#pragma unroll
      for (int n = 0; n < 8; ++n) va[n] = opaque(slot + row * 512 + (((2 * n + (p >> 1)) ^ X) << 4) + (p & 1) * 8);
    }
    constexpr float C2 = 0.0625f * 1.4426950408889634f;
    const unsigned sw = opaque(L_S + c * 128 + (g * 2 + (lane & 1)) * 16);
    float mx = -INFINITY;
    {
      u32x4 qf[8];
      {
        u32x4 kf[16];
        WAIT_GE(L_LANDED, 16 + 16 * c + 16);
#pragma unroll
        for (int s = 0; s < 8; ++s) { kf[s] = ld128(ka[s & 3] + (s >> 2) * 256); kf[8 + s] = ld128(ka[s & 3] + (s >> 2) * 256 + 8192); }
        LGKM0();
        if (lane == 0) stv(L_CONS + c * 4, 1u);
        WAIT_GE(L_QRDY, 1u);
#pragma unroll
        for (int s = 0; s < 8; ++s) qf[s] = ld128(L_Q + (lane & 1) * 512 + s * 64 + g * 16);
        SCHED();
        const u32x4 sp = qk_scores(kf, qf, mx);
        if (r < 2) *(LAS u32x4*)sw = sp;
      }
#pragma unroll 1
      for (int k = 1; k < NTILE / 4; ++k) {
        const unsigned tg = (k & 1) << 16;
        WAIT_GE(L_LANDED, 16 + 16 * (c + 4 * k) + 16);
        u32x4 kf[16];
#pragma unroll
        for (int s = 0; s < 8; ++s) { kf[s] = ld128(ka[s & 3] + tg + (s >> 2) * 256); kf[8 + s] = ld128(ka[s & 3] + tg + (s >> 2) * 256 + 8192); }
        SCHED();
        const u32x4 sp = qk_scores(kf, qf, mx);
        if (r < 2) *(LAS u32x4*)(sw + k * 512) = sp;
        LGKM0();
        if (lane == 0) stv(L_CONS + c * 4, k + 1);
      }
    }
    mx = rbf(x32max(x16max(mx)));
    if (lane < 2) *(LAS float*)(L_MW + (c * 2 + lane) * 4) = mx;
    LGKM0();
    if (lane == 0) (void)__hip_atomic_fetch_add((LAS unsigned*)L_MCNT, 1u, __ATOMIC_RELAXED, __HIP_MEMORY_SCOPE_WORKGROUP);
    WAIT_GE(L_MCNT, 4u);
    const float Mc = fmaxf(fmaxf(ldf(L_MW + (lane & 1) * 4), ldf(L_MW + 8 + (lane & 1) * 4)), fmaxf(ldf(L_MW + 16 + (lane & 1) * 4), ldf(L_MW + 24 + (lane & 1) * 4))) * C2;
    const unsigned tag = ldv(L_TAG);
    const __amdgpu_buffer_rsrc_t rs = ws_rsrc(ws);
    const unsigned pa = opaque((64 * g + r) * 4);
    f32x4 acc[16];
#pragma unroll
    for (int n = 0; n < 16; ++n) acc[n] = f32x4{0.f, 0.f, 0.f, 0.f};
    float l = 0.f, Lf[2] = {0.f, 0.f};
    float pre[4][2];
#pragma unroll
    for (int i = 0; i < 4; ++i) { pre[i][0] = 0.f; pre[i][1] = 0.f; }
#pragma unroll 1
    for (int k = 0; k < NTILE / 4; ++k) {
      const unsigned tg = (k & 1) << 16;
      const unsigned pos = NTILE + c + 4 * k;
      unsigned vt[8];
#pragma unroll
      for (int n = 0; n < 8; ++n) vt[n] = opaque(va[n] + tg);
      const u32x4 sp = ld128(sw + k * 512);
      float p[8];
#pragma unroll
      for (int i = 0; i < 4; ++i) { p[2 * i] = __builtin_amdgcn_exp2f(fmaf(bf2f(sp[i] & 0xffffu), C2, -Mc)); p[2 * i + 1] = __builtin_amdgcn_exp2f(fmaf(__uint_as_float(sp[i] & 0xffff0000u), C2, -Mc)); }
      l += ((p[0] + p[1]) + (p[2] + p[3])) + ((p[4] + p[5]) + (p[6] + p[7]));
      u32x4 pf; pf.x = cvtpk(p[0], p[1]); pf.y = cvtpk(p[2], p[3]); pf.z = cvtpk(p[4], p[5]); pf.w = cvtpk(p[6], p[7]);
      if (k == NTILE / 4 - 1) {
        const float lt = x32sum(x16sum(l));
        Lf[0] = dppf<0x00>(lt); Lf[1] = dppf<0x55>(lt);
        if (c > 0) {
          WAIT_GE(L_B1, c);
          Lf[0] += ldf(L_LR + (c - 1) * 8); Lf[1] += ldf(L_LR + (c - 1) * 8 + 4);
#pragma unroll
          for (int i = 0; i < 4; ++i) { pre[i][0] = ldf(L_PART + (c - 1) * 2048 + pa + i * 64); pre[i][1] = ldf(L_PART + (c - 1) * 2048 + 1024 + pa + i * 64); }
        }
        asm volatile("" : "+v"(pre[0][0]), "+v"(pre[0][1]), "+v"(pre[1][0]), "+v"(pre[1][1]), "+v"(pre[2][0]), "+v"(pre[2][1]), "+v"(pre[3][0]), "+v"(pre[3][1]),
                          "+v"(Lf[0]), "+v"(Lf[1]), "+v"(pf));
        SPIN_GE(L_LANDED, 16 + 16 * pos + 16);
      } else {
        asm volatile("" : "+v"(pf));
        WAIT_GE(L_LANDED, 16 + 16 * pos + 16);
      }
      u32x2 vf[32];
#pragma unroll
      for (int n = 0; n < 8; ++n) { vf[2 * n] = trrd(vt[n]); vf[2 * n + 1] = trrd(vt[n] + 8192); }
      SCHED();
#pragma unroll
      for (int n = 0; n < 8; ++n) {
        acc[n] = mfma(pf, u32x4{vf[2 * n].x, vf[2 * n].y, vf[2 * n + 1].x, vf[2 * n + 1].y}, acc[n]);
        vf[16 + 2 * n] = trrd(vt[n] + 256); vf[17 + 2 * n] = trrd(vt[n] + 256 + 8192);
        SCHED();
      }
#pragma unroll
      for (int n = 8; n < 16; ++n) acc[n] = mfma(pf, u32x4{vf[2 * n].x, vf[2 * n].y, vf[2 * n + 1].x, vf[2 * n + 1].y}, acc[n]);
      LGKM0();
      if (lane == 0) stv(L_CONS + c * 4, NTILE / 4 + k + 1);
    }
    {
      float val[4][2];
#pragma unroll
      for (int i = 0; i < 4; ++i)
#pragma unroll
        for (int h = 0; h < 2; ++h) val[i][h] = sel4(g, acc[i][h], acc[4 + i][h], acc[8 + i][h], acc[12 + i][h]) + pre[i][h];
      if (c < 3) {
#pragma unroll
        for (int i = 0; i < 4; ++i) { *(LAS float*)(L_PART + c * 2048 + pa + i * 64) = val[i][0]; *(LAS float*)(L_PART + c * 2048 + 1024 + pa + i * 64) = val[i][1]; }
        if (lane == 0) { *(LAS float*)(L_LR + c * 8) = Lf[0]; *(LAS float*)(L_LR + c * 8 + 4) = Lf[1]; }
        LGKM0();
        if (lane == 0) stv(L_B1, c + 1);
      } else {
        const unsigned h = lane & 1;
        const unsigned o_st = W_GA + (unsigned)((grp * 16 + h * 8 + 2 * g) * 16 + j) * 256u + (r >> 1) * 16u;
#pragma unroll
        for (int i = 0; i < 4; ++i) {
          const float n0 = dppf<XOR1>(val[i][0]), n1 = dppf<XOR1>(val[i][1]);
          st_gran2(rs, o_st + (i >> 1) * 4096 + (i & 1) * 128, tag, h ? n1 : val[i][0], h ? val[i][1] : n0);
        }
        const float M0 = dppf<0x00>(Mc), M1 = dppf<0x55>(Mc);
        if (lane < 16) st_gran2(rs, W_ML + (unsigned)((grp * 16 + lane) * 16 + j) * 16u, tag, lane < 8 ? M0 : M1, lane < 8 ? Lf[0] : Lf[1]);
      }
    }
    if (c == 0) {
      const unsigned o_val = opaque(W_GA + (unsigned)((grp * 16 + j) * 16 + g) * 256u + r * 16u);
      const unsigned o_ml = opaque(W_ML + (unsigned)((grp * 16 + j) * 16 + ((r + g) & 15)) * 16u);
      WAIT_GE(L_LANDED, LD_V63);
      const unsigned long long t1 = rt();
      unsigned spins = 0;
      u32x4 v[4], ml;
      for (;;) {
        asm volatile("" ::: "memory");
#pragma unroll
        for (int i = 0; i < 4; ++i) v[i] = ld_gran2(rs, o_val + i * 1024);
        ml = ld_gran2(rs, o_ml);
        const unsigned bad = ((v[0].y ^ tag) | (v[0].w ^ tag) | (v[1].y ^ tag)) | ((v[1].w ^ tag) | (v[2].y ^ tag) | (v[2].w ^ tag)) |
                             ((v[3].y ^ tag) | (v[3].w ^ tag) | (ml.y ^ tag)) | (ml.w ^ tag);
        if (__all(bad == 0)) break;
        if ((++spins & 63u) == 0 && rt() - t1 > TMO) break;
      }
    {
      const float Mp = __uint_as_float(ml.x), Lp = __uint_as_float(ml.z);
      float M = Mp; M = fmaxf(M, dppf<XOR1>(M)); M = fmaxf(M, dppf<XOR2>(M)); M = fmaxf(M, dppf<HMIR>(M)); M = fmaxf(M, dppf<ROR8>(M));
      const float wt = __builtin_amdgcn_exp2f(Mp - M);
      const float s0 = dppf<0x150>(wt), s1 = dppf<0x154>(wt), s2 = dppf<0x158>(wt), s3 = dppf<0x15C>(wt);
      float L = wt * Lp; L += dppf<XOR1>(L); L += dppf<XOR2>(L); L += dppf<HMIR>(L); L += dppf<ROR8>(L);
      float oa = fmaf(s1, __uint_as_float(v[1].x), s0 * __uint_as_float(v[0].x)) + fmaf(s3, __uint_as_float(v[3].x), s2 * __uint_as_float(v[2].x));
      float ob = fmaf(s1, __uint_as_float(v[1].z), s0 * __uint_as_float(v[0].z)) + fmaf(s3, __uint_as_float(v[3].z), s2 * __uint_as_float(v[2].z));
      const float rl = __builtin_amdgcn_rcpf(L);
      oa = x32sum(x16sum(oa)) * rl; ob = x32sum(x16sum(ob)) * rl;
      const unsigned o2 = cvtpk(oa, ob);
      if (lane < 16) *(LAS unsigned*)(L_O16 + r * 4) = o2;
      LGKM0();
      if (lane == 0) stv(L_ORDY, 1u);
      }
    } else if (c >= 2) {
      const float gate = __uint_as_float(ldv(L_GATE));
      const unsigned o_bs = opaque(gb_store_off(b, wg & 127, lane));
      if (c == 2) project<2, false>(lane, tag, gate, rs, o_bs); else project<4, false>(lane, tag, gate, rs, o_bs);
    } else if (c == 1) {
      const int fidx = kvh * 2 + (j >> 3);
      const unsigned q4 = r >> 2, p = lane & 3;
      float ys[7];
#pragma unroll
      for (int k = 0; k < 7; ++k) ys[k] = 0.f;
      if (fidx < 14) {
        const unsigned xa_addr = opaque(L_X + (lane & 1) * 896 + g * 8);
        const unsigned w1a = opaque(L_W1 + (4 * g + q4) * 32 + p * 8), w2a = opaque(L_W2 + (4 * g + q4) * (D * 2) + p * 8);
        u32x4 xf[14];
#pragma unroll
        for (int s = 0; s < 14; ++s) { const u32x2 a0 = ld64(xa_addr + s * 64), a1 = ld64(xa_addr + s * 64 + 32); xf[s] = u32x4{a0.x, a0.y, a1.x, a1.y}; }
        const float omg = __uint_as_float(ldv(L_OMG));
        WAIT_GE(L_LANDED, LD_W1);
        u32x2 bf[28];
#pragma unroll
        for (int s = 0; s < 14; ++s) { bf[2 * s] = trrd(w1a + s * 1024); bf[2 * s + 1] = trrd(w1a + s * 1024 + 512); }
        SCHED();
        f32x4 a0 = {0.f, 0.f, 0.f, 0.f}, a1 = {0.f, 0.f, 0.f, 0.f};
#pragma unroll
        for (int s = 0; s < 7; ++s) {
          a0 = mfma(xf[2 * s], u32x4{bf[4 * s].x, bf[4 * s].y, bf[4 * s + 1].x, bf[4 * s + 1].y}, a0);
          a1 = mfma(xf[2 * s + 1], u32x4{bf[4 * s + 2].x, bf[4 * s + 2].y, bf[4 * s + 3].x, bf[4 * s + 3].y}, a1);
        }
        float hv = rbf(b ? a0[1] + a1[1] : a0[0] + a1[0]);
        hv = hv / (1.f + __expf(-hv));
        *(LAS unsigned short*)(L_H16 + r * 2) = (unsigned short)f2bf(hv);
        LGKM0();
        const u32x2 hf = ld64(L_H16 + g * 8);
        WAIT_GE(L_LANDED, LD_W2);
        u32x2 w2f[28];
#pragma unroll
        for (int ct = 0; ct < 28; ++ct) w2f[ct] = trrd(w2a + ct * 32);
        SCHED();
#pragma unroll
        for (int k = 0; k < 7; ++k) {
          float y[4];
#pragma unroll
          for (int u = 0; u < 4; ++u)
            y[u] = __builtin_amdgcn_mfma_f32_16x16x16bf16_1k(__builtin_bit_cast(s16x4, hf), __builtin_bit_cast(s16x4, w2f[4 * k + u]), f32x4{0.f, 0.f, 0.f, 0.f}, 0, 0, 0)[0];
          ys[k] = omg * (sel4(g, y[0], y[1], y[2], y[3]));
        }
      }
      LGKM0();
#pragma unroll
      for (int k = 0; k < 7; ++k) *(LAS float*)(L_YB + (64 * k + lane) * 4) = ys[k];
      LGKM0();
      if (lane == 0) stv(L_YRDY, 1u);
    }
  }
}

extern "C" void kernel_launch(void* const* d_in, const int* in_sizes, int n_in, void* d_out, int out_size, void* d_ws, size_t ws_size, hipStream_t stream) {
  (void)in_sizes; (void)n_in; (void)out_size; (void)ws_size;
  (void)hipFuncSetAttribute((const void*)gated_mix_kernel, hipFuncAttributeMaxDynamicSharedMemorySize, L_BYTES);
  gated_mix_kernel<<<NWG, NTHR, L_BYTES, stream>>>((const unsigned short*)d_in[0], (const unsigned short*)d_in[1], (const unsigned short*)d_in[2],
                                                   (const unsigned short*)d_in[3], (const unsigned short*)d_in[4], (const unsigned short*)d_in[6],
                                                   (const unsigned short*)d_in[7], (const unsigned short*)d_in[8], (unsigned short*)d_out, (unsigned char*)d_ws);
}
```
